```python
import torch

def _softmax(x, axis=-1):
    return torch.softmax(x, dim=axis)
def _gelu(x):
    return torch.nn.functional.gelu(x, approximate="tanh")
def _silu(x):
    return torch.nn.functional.silu(x)
def _sigmoid(x):
    return torch.sigmoid(x)
def _layernorm(x, axis=-1, eps=1e-5):
    m = x.mean(dim=axis, keepdim=True)
    v = x.var(dim=axis, keepdim=True, unbiased=False)
    return (x - m) / torch.sqrt(v + eps)
def _scatter_add(tgt, idx, src, axis=0):
    return tgt.clone().index_add_(axis, idx, src)
def _segment_sum(src, seg, n):
    out = torch.zeros((n,) + src.shape[1:], dtype=src.dtype)
    return out.index_add_(0, seg, src)
def _pad(x, pw):
    flat = tuple(v for pair in reversed(pw) for v in pair)
    return torch.nn.functional.pad(x, flat)

kernel_name = 'standalone__window_attn_swin__vision'

def setup_inputs():

    _w = (lambda a: (lambda t: t.to(torch.bfloat16) if t.is_floating_point() else t)(torch.as_tensor(a)))
    H = W = 224
    C = 96
    ws = 7
    nh = 4
    shift = ws // 2
    coords = torch.stack(torch.meshgrid(torch.arange(ws), torch.arange(ws), indexing='ij'), 0).reshape(2, -1)
    rel = coords[:, :, None] - coords[:, None, :]
    rel[0] += ws - 1
    rel[1] += ws - 1
    rel[0] *= 2 * ws - 1
    rp_idx = rel.sum(0).to(torch.int64)
    img_mask = torch.zeros((1, H, W, 1), dtype=float)
    cnt = 0
    for hs in (slice(0, H - ws), slice(H - ws, H - shift), slice(H - shift, H)):
        for vs in (slice(0, W - ws), slice(W - ws, W - shift), slice(W - shift, W)):
            img_mask[:, hs, vs, :] = cnt
            cnt += 1
    mw = img_mask.reshape(1, H // ws, ws, W // ws, ws, 1).permute(0, 1, 3, 2, 4, 5).reshape(-1, ws * ws)
    am = mw[:, :, None] - mw[:, None, :]
    attn_mask = torch.where(am != 0, -100.0, 0.0).to(float)
    return {
        "x": _w((lambda *s: torch.randn(*s, dtype=torch.bfloat16) * 1.6)(1, H, W, C)),
        "wqkv": _w((lambda *s: torch.randn(*s, dtype=torch.bfloat16) * 1.6)(C, 3 * C) * 0.02),
        "wo": _w((lambda *s: torch.randn(*s, dtype=torch.bfloat16) * 0.05)(C, C) * 0.02),
        "rpb": _w((lambda *s: torch.randn(*s, dtype=torch.bfloat16) * 0.4)((2 * ws - 1) * (2 * ws - 1), nh)),
        "rp_idx": _w(rp_idx),
        "attn_mask": _w(attn_mask),
    }

def reference(x, wqkv, wo, rpb, rp_idx, attn_mask):
    B, H, W, C = x.shape
    N = rp_idx.shape[0]
    ws = int(round(N ** 0.5))
    nh = rpb.shape[-1]
    hd = C // nh
    shift = ws // 2
    xs = torch.roll(x, (-shift, -shift), (1, 2))
    nWh = H // ws
    nWw = W // ws
    xw = (lambda x, *d: x.permute(*d))(xs.reshape(B, nWh, ws, nWw, ws, C), 0, 1, 3, 2, 4, 5).reshape(B * nWh * nWw, N, C)
    qkv = (lambda x, *d: x.permute(*d))(torch.einsum('wnc,cd->wnd', xw, wqkv).reshape(-1, N, 3, nh, hd), 2, 0, 3, 1, 4)
    q, k, v = qkv[0], qkv[1], qkv[2]
    attn = torch.einsum('whid,whjd->whij', q, k) * (hd ** -0.5)
    bias = (lambda x, *d: x.permute(*d))(rpb[rp_idx.reshape(-1)].reshape(N, N, nh), 2, 0, 1)
    attn = attn + bias[None, :, :, :]
    attn = (attn.reshape(B, nWh * nWw, nh, N, N) + attn_mask[None, :, None, :, :]).reshape(-1, nh, N, N)
    attn = _softmax(attn, axis=-1)
    out = (lambda x, *d: x.permute(*d))(torch.einsum('whij,whjd->whid', attn, v), 0, 2, 1, 3).reshape(-1, N, C)
    out = torch.einsum('wnc,cd->wnd', out, wo)
    out = (lambda x, *d: x.permute(*d))(out.reshape(B, nWh, nWw, ws, ws, C), 0, 1, 3, 2, 4, 5).reshape(B, H, W, C)
    return torch.roll(out, (shift, shift), (1, 2))
```

```cpp
#include <hip/hip_runtime.h>
#include <hip/hip_bf16.h>
#include <stdint.h>

typedef __attribute__((ext_vector_type(8))) __bf16 bf16x8;
typedef __attribute__((ext_vector_type(2))) __bf16 bf16x2;
typedef __attribute__((ext_vector_type(4))) float f32x4;
typedef __attribute__((ext_vector_type(2))) float f32x2;
typedef __attribute__((ext_vector_type(4))) uint32_t u32x4;
typedef __attribute__((ext_vector_type(2))) uint32_t u32x2;
typedef __attribute__((ext_vector_type(4))) short s16x4;

#define DEV __device__ __forceinline__
#define LAS __attribute__((address_space(3)))
typedef LAS char* lds_ptr;

constexpr int OFF_X = 0, XS = 9408;
constexpr int OFF_MASK = 37632, MS = 6272;
constexpr int OFF_WIMG = 62720, TS = 3104;
constexpr int OFF_WORAW = 124800;
constexpr int OFF_RPB = 143232;
constexpr int OFF_XL = 145936;
constexpr int OFF_QL = 146704;
constexpr int OFF_ZERO = 148752;
constexpr int OFF_PL = 149008;
constexpr int OFF_OL = 150032;
constexpr int LDS_TOTAL = 156176 + 64;
constexpr int OFF_WOFRAG = OFF_WIMG;
constexpr int OFF_OB = OFF_WIMG + 18432;
constexpr int OBS = 9216;

constexpr float LOG2E = 1.4426950408889634f;
constexpr float QSCALE = 0.20412414523193154f * 1.4426950408889634f;
constexpr float NEG_BIG = -1.0e30f;

DEV uint32_t pk(float a, float b) {
    f32x2 v = {a, b};
    bf16x2 r = __builtin_convertvector(v, bf16x2);
    return __builtin_bit_cast(uint32_t, r);
}
DEV f32x4 mfma(u32x4 a, u32x4 b, f32x4 c) {
    return __builtin_amdgcn_mfma_f32_16x16x32_bf16(__builtin_bit_cast(bf16x8, a), __builtin_bit_cast(bf16x8, b), c, 0, 0, 0);
}
DEV u32x4 lds_r128(lds_ptr p) { return *(const LAS u32x4*)p; }
DEV u32x2 lds_r64(lds_ptr p) { return *(const LAS u32x2*)p; }
DEV uint32_t lds_r16(lds_ptr p) { return (uint32_t) * (const LAS uint16_t*)p; }
DEV void lds_w128(lds_ptr p, u32x4 v) { *(LAS u32x4*)p = v; }
DEV void lds_w32(lds_ptr p, uint32_t v) { *(LAS uint32_t*)p = v; }
DEV u32x2 lds_tr(lds_ptr p) {
    return __builtin_bit_cast(u32x2, __builtin_amdgcn_ds_read_tr16_b64_v4i16((LAS s16x4*)p));
}
DEV float bflo(uint32_t v) { return __builtin_bit_cast(float, v << 16); }
DEV float bfhi(uint32_t v) { return __builtin_bit_cast(float, v & 0xffff0000u); }
DEV uint32_t scale2(uint32_t v) { return pk(bflo(v) * QSCALE, bfhi(v) * QSCALE); }
DEV float ex2(float v) { return __builtin_amdgcn_exp2f(v); }

DEV void dma16(const void* sbase, uint32_t voff, uint32_t lds_dst) {
    unsigned keep;
    asm volatile("s_mov_b32 %0, m0\n\ts_mov_b32 m0, %3\n\ts_nop 0\n\tglobal_load_lds_dwordx4 %1, %2\n\ts_mov_b32 m0, %0"
                 : "=&s"(keep) : "v"(voff), "s"(sbase), "s"(lds_dst) : "memory");
}
#define BARRIER() do { asm volatile("s_waitcnt lgkmcnt(0)" ::: "memory"); __builtin_amdgcn_s_barrier(); asm volatile("" ::: "memory"); } while (0)
#define WAIT_VM(n) asm volatile("s_waitcnt vmcnt(" #n ")" ::: "memory")

struct Frags {
    u32x4 Q[3], K[4];
    u32x4 VT[2][2];
};

DEV void pv_norm(const Frags& F, u32x4 P0, u32x4 P1, uint32_t (&o)[3], uint32_t& sumbits) {
    f32x4 zero = {0.f, 0.f, 0.f, 0.f};
    f32x4 oa = mfma(F.VT[0][0], P0, zero);
    f32x4 ob = mfma(F.VT[1][0], P0, zero);
    oa = mfma(F.VT[0][1], P1, oa);
    ob = mfma(F.VT[1][1], P1, ob);
    float sum = ob[2];
    sumbits = __builtin_bit_cast(uint32_t, sum);
    float rs = __builtin_amdgcn_rcpf(sum);
    o[0] = pk(oa[0] * rs, oa[1] * rs);
    o[1] = pk(oa[2] * rs, oa[3] * rs);
    o[2] = pk(ob[0] * rs, ob[1] * rs);
}

template <bool SAFE>
DEV void attn_tile(const Frags& F, int qt, const f32x4 (&bias)[3], float bias48, bool gsel, lds_ptr M, int ma0, int ma1, int ma2,
                   int m48a, u32x4 IHL, uint32_t (&o)[3], uint32_t& sumbits) {
    float m48 = bflo(lds_r16(M + m48a));
    float c3 = __builtin_fmaf(m48, LOG2E, bias48);
    c3 = gsel ? c3 : NEG_BIG;
    f32x4 z3 = {c3, 0.f, 0.f, 0.f};
    f32x4 s0 = mfma(F.K[0], F.Q[qt], bias[0]);
    f32x4 s1 = mfma(F.K[1], F.Q[qt], bias[1]);
    f32x4 s2 = mfma(F.K[2], F.Q[qt], bias[2]);
    f32x4 s3 = mfma(F.K[3], F.Q[qt], z3);
    float s30 = s3[0];
    if (!SAFE) {
        u32x4 m0 = lds_r128(M + ma0), m1 = lds_r128(M + ma1), m2 = lds_r128(M + ma2);
        s0 = mfma(IHL, m0, s0);
        s1 = mfma(IHL, m1, s1);
        s2 = mfma(IHL, m2, s2);
    } else {
        const int lane = threadIdx.x & 63;
        const int g = lane >> 4, mm = lane & 15;
        const int v = (mm >> 1) & 7;
        u32x2 mv0 = lds_r64(M + 128 * mm + 16 * ((0 + (g >> 1)) ^ v) + 8 * (g & 1));
        u32x2 mv1 = lds_r64(M + 128 * mm + 16 * ((2 + (g >> 1)) ^ v) + 8 * (g & 1));
        u32x2 mv2 = lds_r64(M + 128 * mm + 16 * ((4 + (g >> 1)) ^ v) + 8 * (g & 1));
#define ADDM(s, mv) s[0] = __builtin_fmaf(bflo(mv[0]), LOG2E, s[0]); s[1] = __builtin_fmaf(bfhi(mv[0]), LOG2E, s[1]); \
                    s[2] = __builtin_fmaf(bflo(mv[1]), LOG2E, s[2]); s[3] = __builtin_fmaf(bfhi(mv[1]), LOG2E, s[3]);
        ADDM(s0, mv0) ADDM(s1, mv1) ADDM(s2, mv2)
#undef ADDM
        s30 = gsel ? s30 : -INFINITY;
        float mx = s30;
#pragma unroll
        for (int r = 0; r < 4; ++r) mx = fmaxf(mx, fmaxf(s0[r], fmaxf(s1[r], s2[r])));
        mx = fmaxf(mx, __shfl_xor(mx, 16));
        mx = fmaxf(mx, __shfl_xor(mx, 32));
#pragma unroll
        for (int r = 0; r < 4; ++r) { s0[r] -= mx; s1[r] -= mx; s2[r] -= mx; }
        s30 -= mx;
    }
    u32x4 P0 = {pk(ex2(s0[0]), ex2(s0[1])), pk(ex2(s0[2]), ex2(s0[3])), pk(ex2(s1[0]), ex2(s1[1])), pk(ex2(s1[2]), ex2(s1[3]))};
    u32x4 P1 = {pk(ex2(s2[0]), ex2(s2[1])), pk(ex2(s2[2]), ex2(s2[3])), pk(ex2(s30), 0.f), 0u};
    pv_norm(F, P0, P1, o, sumbits);
}

template <bool SAFE>
DEV void attn_left(const Frags& F, float cinit, bool valid, lds_ptr QLw, const int (&qa)[4], lds_ptr PL, uint32_t (&o)[3],
                   uint32_t& sumbits) {
    const int lane = threadIdx.x & 63;
    const int g = lane >> 4;
    f32x4 s = {cinit, 0.f, 0.f, 0.f};
#pragma unroll
    for (int kt = 0; kt < 4; ++kt) s = mfma(lds_r128(QLw + qa[kt]), F.K[kt], s);
    float v = s[0];
    if (SAFE) {
        v = valid ? v : -INFINITY;
        float mx = v;
#pragma unroll
        for (int d = 1; d < 64; d <<= 1) mx = fmaxf(mx, __shfl_xor(mx, d));
        v -= mx;
    }
    *(LAS uint16_t*)(PL + 2 * lane) = (uint16_t)pk(ex2(v), 0.f);
    u32x2 a0 = lds_r64(PL + 8 * g), a1 = lds_r64(PL + 8 * g + 32), a2 = lds_r64(PL + 8 * g + 64), a3 = lds_r64(PL + 8 * g + 96);
    u32x4 P0 = {a0[0], a0[1], a1[0], a1[1]}, P1 = {a2[0], a2[1], a3[0], a3[1]};
    pv_norm(F, P0, P1, o, sumbits);
}

__global__ __launch_bounds__(512, 2) void wattn_kernel(const uint16_t* __restrict__ x, const uint16_t* __restrict__ wqkv,
                                                       const uint16_t* __restrict__ wo, const uint16_t* __restrict__ rpb,
                                                       const int* __restrict__ rpidx, const uint16_t* __restrict__ mask,
                                                       uint16_t* __restrict__ out) {
    extern __shared__ __attribute__((aligned(16))) char smem_[];
    lds_ptr L = (lds_ptr)smem_;
    const uint32_t Lbase = (uint32_t)(uintptr_t)L;
    const int tid = threadIdx.x, lane = tid & 63;
    const int wv = __builtin_amdgcn_readfirstlane(tid >> 6);
    const int h = wv & 3, p = wv >> 2;
    const int g = lane >> 4, m = lane & 15;
    const int blk = blockIdx.x;
    const int wh = blk >> 3, ww0 = (blk & 7) * 4;

    u32x4 wreg[9];
    int wrow[3], wj[3];
    if (wv < 6) {
#pragma unroll
        for (int i = 0; i < 3; ++i) {
            int c = 64 * i + lane;
            int r = c / 12;
            wj[i] = c - 12 * r;
            wrow[i] = 16 * wv + r;
        }
#pragma unroll
        for (int part = 0; part < 3; ++part)
#pragma unroll
            for (int i = 0; i < 3; ++i)
                wreg[part * 3 + i] = *(const u32x4*)((const char*)wqkv + wrow[i] * 576 + part * 192 + wj[i] * 16);
    }
    const int bA = lane / 12, bB = (64 + lane) / 12;
    auto load_x = [&](int i) {
        const int ww = ww0 + i;
        const int x0 = 7 * ww + 3;
        const int wrapA = (ww == 31 && bA >= 4) ? -43008 : 0;
        const int wrapB = (ww == 31) ? -43008 : 0;
        for (int a = h; a < 7; a += 4) {
            int y = 7 * wh + a + 3;
            if (y >= 224) y -= 224;
            const int rowbase = (y * 224 + x0) * 192;
            const int tA = 7 * a + bA, tB = 7 * a + bB;
            const uint32_t offA = (uint32_t)(rowbase + wrapA + ((16 * lane) ^ ((tA << 2) & 32)));
            const uint32_t offB = (uint32_t)(rowbase + wrapB + ((1024 + 16 * lane) ^ ((tB << 2) & 32)));
            const uint32_t dst = Lbase + OFF_X + i * XS + a * 1344;
            dma16(x, offA, dst);
            if (lane < 20) dma16(x, offB, dst + 1024);
        }
    };
    load_x(p);
    if (wv == 7) {
        int y = 7 * wh + 9; if (y >= 224) y -= 224;
        int xx = 7 * (ww0 + bA) + 9; if (xx >= 224) xx -= 224;
        if (lane < 48) dma16(x, (uint32_t)((y * 224 + xx) * 192 + 16 * (lane - 12 * bA)), Lbase + OFF_XL);
    }
    u32x4 idxv[3][3];
    int idx48[3], idxL;
#pragma unroll
    for (int qt = 0; qt < 3; ++qt) {
        const int q = 16 * qt + m;
#pragma unroll
        for (int kt = 0; kt < 3; ++kt) idxv[qt][kt] = *(const u32x4*)(rpidx + q * 49 + 16 * kt + 4 * g);
        idx48[qt] = rpidx[q * 49 + 48];
    }
    idxL = rpidx[48 * 49 + (lane < 48 ? lane : 48)];
    const int mc0 = 64 * h + lane, mc1 = 256 + 64 * h + lane;
    const int mq0 = mc0 / 7, mk0 = mc0 - 7 * mq0, mq1 = mc1 / 7, mk1 = mc1 - 7 * mq1;
    const int msrc0 = 98 * mq0 + (mk0 < 6 ? 16 * mk0 : 82), msrc1 = 98 * mq1 + (mk1 < 6 ? 16 * mk1 : 82);
    const int mdst0 = 128 * mq0 + 16 * (mk0 ^ ((mq0 >> 1) & 7)), mdst1 = 128 * mq1 + 16 * (mk1 ^ ((mq1 >> 1) & 7));
    u32x4 mreg0, mreg1 = {0, 0, 0, 0};
    auto load_mask = [&](int i) {
        const char* mb = (const char*)mask + (size_t)(4 * blk + i) * 4802;
        mreg0 = *(const u32x4*)(mb + msrc0);
        if (mc1 < 343) mreg1 = *(const u32x4*)(mb + msrc1);
    };
    auto store_mask = [&](int i) {
        lds_w128(L + OFF_MASK + i * MS + mdst0, mreg0);
        if (mc1 < 343) lds_w128(L + OFF_MASK + i * MS + mdst1, mreg1);
    };
    load_mask(p);

    if (wv >= 6) {
#pragma unroll
        for (int k = 0; k < 9; ++k) {
            const int kk = (wv - 6) * 9 + k;
            dma16(wo, kk * 1024 + lane * 16, Lbase + OFF_WORAW + kk * 1024);
        }
#pragma unroll
        for (int i = 0; i < 6; ++i) {
            const int e = tid - 384 + 128 * i;
            if (e < 676) *(LAS float*)(L + OFF_RPB + 4 * e) = bflo((uint32_t)rpb[e]) * LOG2E;
        }
        if (wv == 6) lds_w32(L + OFF_ZERO + 4 * lane, 0u);
    } else {
#pragma unroll
        for (int i = 0; i < 3; ++i) {
            const int row = wrow[i];
            const int prow = row ^ (((row >> 3) & 1) << 2);
            const int hh = wj[i] / 3, piece = wj[i] - 3 * hh;
            lds_ptr dq = L + OFF_WIMG + (hh * 5 + piece) * TS + 32 * prow;
            u32x4 q = wreg[i], k = wreg[3 + i], v = wreg[6 + i];
            lds_w32(dq + 0, scale2(q[0]));
            lds_w32(dq + 8, scale2(q[1]));
            lds_w32(dq + 16, scale2(q[2]));
            lds_w32(dq + 24, scale2(q[3]));
            lds_w32(dq + 4, k[0]);
            lds_w32(dq + 12, k[1]);
            lds_w32(dq + 20, k[2]);
            lds_w32(dq + 28, k[3]);
            if (piece < 2) {
                lds_w128(L + OFF_WIMG + (hh * 5 + 3) * TS + 32 * prow + 16 * piece, v);
            } else {
                lds_ptr dv = L + OFF_WIMG + (hh * 5 + 4) * TS + 32 * prow;
                u32x4 a = {v[0], 0u, v[1], 0u}, b = {v[2], 0u, v[3], 0u};
                lds_w128(dv, a);
                lds_w128(dv + 16, b);
            }
        }
    }
    store_mask(p);
    WAIT_VM(0);
    BARRIER();

    load_x(p + 2);
    load_mask(p + 2);

    u32x4 Wf[5][3];
    {
        const int q4 = m >> 2, p4 = m & 3;
        lds_ptr a1 = L + OFF_WIMG + h * 5 * TS + 32 * (8 * g + q4 + 4 * (g & 1)) + 8 * p4;
        lds_ptr a2 = L + OFF_WIMG + h * 5 * TS + 32 * (8 * g + q4 + 4 * (1 - (g & 1))) + 8 * p4;
#pragma unroll
        for (int t = 0; t < 5; ++t)
#pragma unroll
            for (int s = 0; s < 3; ++s) {
                u32x2 lo = lds_tr(a1 + t * TS + 1024 * s), hi = lds_tr(a2 + t * TS + 1024 * s);
                Wf[t][s] = u32x4{lo[0], lo[1], hi[0], hi[1]};
            }
    }
    const float onec = ((m & 3) >= 2) ? 1.0f : 0.0f;
    const f32x4 ONES = {onec, onec, onec, onec};
    const f32x4 ZERO = {0.f, 0.f, 0.f, 0.f};
    auto project = [&](lds_ptr xa, u32x4& Q, u32x4& K, u32x2& V0, u32x2& V1) {
        u32x4 xf0 = lds_r128(xa), xf1 = lds_r128(xa + 64), xf2 = lds_r128(xa + 128);
        f32x4 a0 = mfma(Wf[0][0], xf0, ZERO), a1 = mfma(Wf[1][0], xf0, ZERO), a2 = mfma(Wf[2][0], xf0, ZERO);
        f32x4 v0 = mfma(xf0, Wf[3][0], ZERO), v1 = mfma(xf0, Wf[4][0], ONES);
        a0 = mfma(Wf[0][1], xf1, a0); a1 = mfma(Wf[1][1], xf1, a1); a2 = mfma(Wf[2][1], xf1, a2);
        v0 = mfma(xf1, Wf[3][1], v0); v1 = mfma(xf1, Wf[4][1], v1);
        a0 = mfma(Wf[0][2], xf2, a0); a1 = mfma(Wf[1][2], xf2, a1); a2 = mfma(Wf[2][2], xf2, a2);
        v0 = mfma(xf2, Wf[3][2], v0); v1 = mfma(xf2, Wf[4][2], v1);
        Q = u32x4{pk(a0[0], a0[1]), pk(a1[0], a1[1]), pk(a2[0], a2[1]), 0u};
        K = u32x4{pk(a0[2], a0[3]), pk(a1[2], a1[3]), pk(a2[2], a2[3]), 0u};
        V0 = u32x2{pk(v0[0], v0[1]), pk(v0[2], v0[3])};
        V1 = u32x2{pk(v1[0], v1[1]), pk(v1[2], v1[3])};
    };
    u32x4 KL;
    u32x2 V0L, V1L;
    lds_ptr QL = L + OFF_QL + wv * 256;
    {
        u32x4 QLr;
        project(L + OFF_XL + 192 * (m >> 2) + 16 * g, QLr, KL, V0L, V1L);
        if ((m & 3) == 0) lds_w128(QL + (m >> 2) * 64 + g * 16, QLr);
    }
    f32x4 bias[3][3];
    float bias48[3], biasL;
#pragma unroll
    for (int qt = 0; qt < 3; ++qt) {
#pragma unroll
        for (int kt = 0; kt < 3; ++kt)
#pragma unroll
            for (int r = 0; r < 4; ++r) bias[qt][kt][r] = *(const LAS float*)(L + OFF_RPB + 16 * idxv[qt][kt][r] + 4 * h);
        bias48[qt] = *(const LAS float*)(L + OFF_RPB + 16 * idx48[qt] + 4 * h);
    }
    biasL = *(const LAS float*)(L + OFF_RPB + 16 * idxL + 4 * h);
    BARRIER();
    {
        const int q4 = m >> 2, p4 = m & 3;
        for (int f = wv; f < 18; f += 8) {
            const int t = f / 3, s = f - 3 * t;
            const int d = (s == 0) ? 4 * g + (q4 & 1) : (s == 1) ? 4 * g + 2 + (q4 & 1) : 16 + 2 * g + (q4 & 1);
            const int r1 = 24 * (((q4 >> 1)) ^ (g & 1)) + d;
            const int r2 = 24 * ((2 + (q4 >> 1)) ^ (g & 1)) + d;
            const int col = (32 * (t >> 1) + 8 * p4 + 4 * (t & 1)) * 2;
            u32x2 lo = lds_tr(L + OFF_WORAW + r1 * 192 + col), hi = lds_tr(L + OFF_WORAW + r2 * 192 + col);
            lds_w128(L + OFF_WOFRAG + f * 1024 + lane * 16, u32x4{lo[0], lo[1], hi[0], hi[1]});
        }
    }
    u32x4 IHL;
    {
        const uint32_t val = (g < 2) ? 0x3FB9u : 0xBB2Cu;
        const int j = m - 8 * (g & 1);
#pragma unroll
        for (int r = 0; r < 4; ++r) IHL[r] = (j == 2 * r) ? val : (j == 2 * r + 1) ? (val << 16) : 0u;
    }
    const int xsw = (16 * g) ^ (((m >> 3) & 1) << 5);
    const int vsw = (m >> 1) & 7;
    const int ma0 = 128 * m + 16 * ((0 + (g & 1)) ^ vsw), ma1 = 128 * m + 16 * ((2 + (g & 1)) ^ vsw), ma2 = 128 * m + 16 * ((4 + (g & 1)) ^ vsw);
    const int m48a = 128 * m + 16 * (6 ^ vsw) + 14;
    const int mLa = 128 * 48 + (lane < 48 ? 2 * lane : 110);
    int qa[4];
#pragma unroll
    for (int kt = 0; kt < 4; ++kt) qa[kt] = ((m >> 2) == kt) ? 16 * g : (OFF_ZERO - OFF_QL - wv * 256 + 16 * g);
    lds_ptr PL = L + OFF_PL + wv * 128;
    const int role = (h + 2 * p) & 3;

    for (int it = 0; it < 2; ++it) {
        const int i = p + 2 * it;
        const int ww = ww0 + i;
        lds_ptr X = L + OFF_X + i * XS;
        lds_ptr M = L + OFF_MASK + i * MS;
        lds_ptr OB = L + OFF_OB + i * OBS;
        lds_ptr OL = L + OFF_OL + p * 3072;
        Frags F;
        {
            u32x2 V0[3], V1[3];
#pragma unroll
            for (int tt = 0; tt < 3; ++tt) project(X + 192 * (16 * tt + m) + xsw, F.Q[tt], F.K[tt], V0[tt], V1[tt]);
            F.K[3] = KL;
            F.VT[0][0] = u32x4{V0[0][0], V0[0][1], V0[1][0], V0[1][1]};
            F.VT[0][1] = u32x4{V0[2][0], V0[2][1], V0L[0], V0L[1]};
            F.VT[1][0] = u32x4{V1[0][0], V1[0][1], V1[1][0], V1[1][1]};
            F.VT[1][1] = u32x4{V1[2][0], V1[2][1], V1L[0], V1L[1]};
        }
        uint32_t o[4][3], sb[4];
        const bool gsel = (g == i);
        const bool validL = (lane < 48) || (lane == 48 + 4 * i);
        const float cL = validL ? __builtin_fmaf(bflo(lds_r16(M + mLa)), LOG2E, biasL) : NEG_BIG;
#pragma unroll
        for (int qt = 0; qt < 3; ++qt)
            attn_tile<false>(F, qt, bias[qt], bias48[qt], gsel, M + 2048 * qt, ma0, ma1, ma2, m48a, IHL, o[qt], sb[qt]);
        attn_left<false>(F, cL, validL, QL + 64 * i, qa, PL, o[3], sb[3]);
        {
            const uint32_t lo = min(min(sb[0], sb[1]), min(sb[2], sb[3])), hi = max(max(sb[0], sb[1]), max(sb[2], sb[3]));
            const bool bad = (lo < 0x2B800000u) || (hi > 0x53800000u);
            if (__builtin_amdgcn_ballot_w64(bad) != 0ull) {
#pragma unroll
                for (int qt = 0; qt < 3; ++qt)
                    attn_tile<true>(F, qt, bias[qt], bias48[qt], gsel, M + 2048 * qt, ma0, ma1, ma2, m48a, IHL, o[qt], sb[qt]);
                attn_left<true>(F, cL, validL, QL + 64 * i, qa, PL, o[3], sb[3]);
            }
        }
        {
            const int osw = lane * 16 + 4 * (h ^ (g & 1));
#pragma unroll
            for (int qt = 0; qt < 3; ++qt)
#pragma unroll
                for (int s = 0; s < 3; ++s) lds_w32(OB + osw + qt * 3072 + s * 1024, o[qt][s]);
            if (m == it) {
#pragma unroll
                for (int s = 0; s < 3; ++s) lds_w32(OL + osw + s * 1024, o[3][s]);
            }
        }
        if (it == 0) { WAIT_VM(0); store_mask(p + 2); }
        BARRIER();
        if (role < 3 || it == 1) {
            lds_ptr orr = (role < 3 ? OB + role * 3072 : OL) + lane * 16;
            u32x4 of0 = lds_r128(orr), of1 = lds_r128(orr + 1024), of2 = lds_r128(orr + 2048);
            const int tok = (role < 3) ? 16 * role + m : 48;
            const int wwo = (role < 3) ? ww : ww0 + p + 2 * (m & 1);
            const int a = tok / 7, b = tok - 7 * a;
            int y = 7 * wh + a + 3; if (y >= 224) y -= 224;
            int xx = 7 * wwo + b + 3; if (xx >= 224) xx -= 224;
            char* dst = (char*)out + (y * 224 + xx) * 192 + 16 * g;
            lds_ptr wf = L + OFF_WOFRAG + lane * 16;
#pragma unroll
            for (int i2 = 0; i2 < 3; ++i2) {
                f32x4 c0 = mfma(lds_r128(wf + (6 * i2 + 0) * 1024), of0, ZERO);
                f32x4 c1 = mfma(lds_r128(wf + (6 * i2 + 3) * 1024), of0, ZERO);
                c0 = mfma(lds_r128(wf + (6 * i2 + 1) * 1024), of1, c0);
                c1 = mfma(lds_r128(wf + (6 * i2 + 4) * 1024), of1, c1);
                c0 = mfma(lds_r128(wf + (6 * i2 + 2) * 1024), of2, c0);
                c1 = mfma(lds_r128(wf + (6 * i2 + 5) * 1024), of2, c1);
                u32x4 r = {pk(c0[0], c0[1]), pk(c0[2], c0[3]), pk(c1[0], c1[1]), pk(c1[2], c1[3])};
                if (role < 3 || m < 2) *(u32x4*)(dst + 64 * i2) = r;
            }
        }
    }
}

extern "C" void kernel_launch(void* const* d_in, const int* in_sizes, int n_in, void* d_out, int out_size, void* d_ws,
                              size_t ws_size, hipStream_t stream) {
    (void)in_sizes; (void)n_in; (void)out_size; (void)d_ws; (void)ws_size;
    (void)hipFuncSetAttribute((const void*)wattn_kernel, hipFuncAttributeMaxDynamicSharedMemorySize, LDS_TOTAL);
    wattn_kernel<<<256, 512, LDS_TOTAL, stream>>>((const uint16_t*)d_in[0], (const uint16_t*)d_in[1], (const uint16_t*)d_in[2],
                                                  (const uint16_t*)d_in[3], (const int*)d_in[4], (const uint16_t*)d_in[5],
                                                  (uint16_t*)d_out);
}
```

```cpp
#include <hip/hip_runtime.h>
#include <hip/hip_bf16.h>
#include <stdint.h>

typedef __attribute__((ext_vector_type(8))) __bf16 bf16x8;
typedef __attribute__((ext_vector_type(2))) __bf16 bf16x2;
typedef __attribute__((ext_vector_type(4))) float f32x4;
typedef __attribute__((ext_vector_type(2))) float f32x2;
typedef __attribute__((ext_vector_type(4))) uint32_t u32x4;
typedef __attribute__((ext_vector_type(2))) uint32_t u32x2;
typedef __attribute__((ext_vector_type(4))) short s16x4;

#define DEV __device__ __forceinline__
#define LAS __attribute__((address_space(3)))
typedef LAS char* lds_ptr;

constexpr int OFF_X = 0, XS = 9408;
constexpr int OFF_MASK = 37632, MS = 6272;
constexpr int OFF_WIMG = 62720, TS = 3104;
constexpr int OFF_WORAW = 124800;
constexpr int OFF_RPB = 143232;
constexpr int OFF_XL = 146048;
constexpr int OFF_QL = 146816;
constexpr int OFF_ZERO = 148864;
constexpr int OFF_PL = 149120;
constexpr int OFF_OL = 150144;
constexpr int LDS_TOTAL = 156288 + 64;
constexpr int OFF_WOFRAG = OFF_WIMG;
constexpr int OFF_OB = OFF_WIMG + 18432;
constexpr int OBS = 9216;

constexpr float LOG2E = 1.4426950408889634f;
constexpr float QSCALE = 0.20412414523193154f * 1.4426950408889634f;
constexpr float NEG_BIG = -1.0e30f;

DEV uint32_t pk(float a, float b) {
    f32x2 v = {a, b};
    bf16x2 r = __builtin_convertvector(v, bf16x2);
    return __builtin_bit_cast(uint32_t, r);
}
DEV f32x4 mfma(u32x4 a, u32x4 b, f32x4 c) {
    return __builtin_amdgcn_mfma_f32_16x16x32_bf16(__builtin_bit_cast(bf16x8, a), __builtin_bit_cast(bf16x8, b), c, 0, 0, 0);
}
DEV u32x4 lds_r128(lds_ptr p) { return *(const LAS u32x4*)p; }
DEV u32x2 lds_r64(lds_ptr p) { return *(const LAS u32x2*)p; }
DEV uint32_t lds_r16(lds_ptr p) { return (uint32_t) * (const LAS uint16_t*)p; }
DEV void lds_w128(lds_ptr p, u32x4 v) { *(LAS u32x4*)p = v; }
DEV void lds_w32(lds_ptr p, uint32_t v) { *(LAS uint32_t*)p = v; }
DEV u32x2 lds_tr(lds_ptr p) {
    return __builtin_bit_cast(u32x2, __builtin_amdgcn_ds_read_tr16_b64_v4i16((LAS s16x4*)p));
}
DEV float bflo(uint32_t v) { return __builtin_bit_cast(float, v << 16); }
DEV float bfhi(uint32_t v) { return __builtin_bit_cast(float, v & 0xffff0000u); }
DEV uint32_t scale2(uint32_t v) { return pk(bflo(v) * QSCALE, bfhi(v) * QSCALE); }
DEV float ex2(float v) { return __builtin_amdgcn_exp2f(v); }

DEV void dma16(const void* sbase, uint32_t voff, uint32_t lds_dst) {
    unsigned keep;
    asm volatile("s_mov_b32 %0, m0\n\ts_mov_b32 m0, %3\n\ts_nop 0\n\tglobal_load_lds_dwordx4 %1, %2\n\ts_mov_b32 m0, %0"
                 : "=&s"(keep) : "v"(voff), "s"(sbase), "s"(lds_dst) : "memory");
}
#define BARRIER() do { asm volatile("s_waitcnt lgkmcnt(0)" ::: "memory"); __builtin_amdgcn_s_barrier(); asm volatile("" ::: "memory"); } while (0)
#define WAIT_VM(n) asm volatile("s_waitcnt vmcnt(" #n ")" ::: "memory")

struct Frags {
    u32x4 Q[3], K[4];
    u32x4 VT[2][2];
};

DEV void pv_norm(const Frags& F, u32x4 P0, u32x4 P1, uint32_t (&o)[3], uint32_t& sumbits) {
    f32x4 zero = {0.f, 0.f, 0.f, 0.f};
    f32x4 oa = mfma(F.VT[0][0], P0, zero);
    f32x4 ob = mfma(F.VT[1][0], P0, zero);
    oa = mfma(F.VT[0][1], P1, oa);
    ob = mfma(F.VT[1][1], P1, ob);
    float sum = ob[2];
    sumbits = __builtin_bit_cast(uint32_t, sum);
    float rs = __builtin_amdgcn_rcpf(sum);
    o[0] = pk(oa[0] * rs, oa[1] * rs);
    o[1] = pk(oa[2] * rs, oa[3] * rs);
    o[2] = pk(ob[0] * rs, ob[1] * rs);
}

template <bool SAFE>
DEV void attn_tile(const Frags& F, int qt, const f32x4 (&bias)[3], float bias48, bool gsel, lds_ptr M, int ma0, int ma1, int ma2,
                   int m48a, u32x4 IHL, uint32_t (&o)[3], uint32_t& sumbits) {
    float m48 = bflo(lds_r16(M + m48a));
    float c3 = __builtin_fmaf(m48, LOG2E, bias48);
    c3 = gsel ? c3 : NEG_BIG;
    f32x4 z3 = {c3, 0.f, 0.f, 0.f};
    f32x4 s0 = mfma(F.K[0], F.Q[qt], bias[0]);
    f32x4 s1 = mfma(F.K[1], F.Q[qt], bias[1]);
    f32x4 s2 = mfma(F.K[2], F.Q[qt], bias[2]);
    f32x4 s3 = mfma(F.K[3], F.Q[qt], z3);
    float s30 = s3[0];
    if (!SAFE) {
        u32x4 m0 = lds_r128(M + ma0), m1 = lds_r128(M + ma1), m2 = lds_r128(M + ma2);
        s0 = mfma(IHL, m0, s0);
        s1 = mfma(IHL, m1, s1);
        s2 = mfma(IHL, m2, s2);
    } else {
        const int lane = threadIdx.x & 63;
        const int g = lane >> 4, mm = lane & 15;
        const int v = (mm >> 1) & 7;
        u32x2 mv0 = lds_r64(M + 128 * mm + 16 * ((0 + (g >> 1)) ^ v) + 8 * (g & 1));
        u32x2 mv1 = lds_r64(M + 128 * mm + 16 * ((2 + (g >> 1)) ^ v) + 8 * (g & 1));
        u32x2 mv2 = lds_r64(M + 128 * mm + 16 * ((4 + (g >> 1)) ^ v) + 8 * (g & 1));
#define ADDM(s, mv) s[0] = __builtin_fmaf(bflo(mv[0]), LOG2E, s[0]); s[1] = __builtin_fmaf(bfhi(mv[0]), LOG2E, s[1]); \
                    s[2] = __builtin_fmaf(bflo(mv[1]), LOG2E, s[2]); s[3] = __builtin_fmaf(bfhi(mv[1]), LOG2E, s[3]);
        ADDM(s0, mv0) ADDM(s1, mv1) ADDM(s2, mv2)
#undef ADDM
        s30 = gsel ? s30 : -INFINITY;
        float mx = s30;
#pragma unroll
        for (int r = 0; r < 4; ++r) mx = fmaxf(mx, fmaxf(s0[r], fmaxf(s1[r], s2[r])));
        mx = fmaxf(mx, __shfl_xor(mx, 16));
        mx = fmaxf(mx, __shfl_xor(mx, 32));
#pragma unroll
        for (int r = 0; r < 4; ++r) { s0[r] -= mx; s1[r] -= mx; s2[r] -= mx; }
        s30 -= mx;
    }
    u32x4 P0 = {pk(ex2(s0[0]), ex2(s0[1])), pk(ex2(s0[2]), ex2(s0[3])), pk(ex2(s1[0]), ex2(s1[1])), pk(ex2(s1[2]), ex2(s1[3]))};
    u32x4 P1 = {pk(ex2(s2[0]), ex2(s2[1])), pk(ex2(s2[2]), ex2(s2[3])), pk(ex2(s30), 0.f), 0u};
    pv_norm(F, P0, P1, o, sumbits);
}

template <bool SAFE>
DEV void attn_left(const Frags& F, float cinit, bool valid, lds_ptr QLw, const int (&qa)[4], lds_ptr PL, uint32_t (&o)[3],
                   uint32_t& sumbits) {
    const int lane = threadIdx.x & 63;
    const int g = lane >> 4;
    f32x4 s = {cinit, 0.f, 0.f, 0.f};
#pragma unroll
    for (int kt = 0; kt < 4; ++kt) s = mfma(lds_r128(QLw + qa[kt]), F.K[kt], s);
    float v = s[0];
    if (SAFE) {
        v = valid ? v : -INFINITY;
        float mx = v;
#pragma unroll
        for (int d = 1; d < 64; d <<= 1) mx = fmaxf(mx, __shfl_xor(mx, d));
        v -= mx;
    }
    *(LAS uint16_t*)(PL + 2 * lane) = (uint16_t)pk(ex2(v), 0.f);
    u32x2 a0 = lds_r64(PL + 8 * g), a1 = lds_r64(PL + 8 * g + 32), a2 = lds_r64(PL + 8 * g + 64), a3 = lds_r64(PL + 8 * g + 96);
    u32x4 P0 = {a0[0], a0[1], a1[0], a1[1]}, P1 = {a2[0], a2[1], a3[0], a3[1]};
    pv_norm(F, P0, P1, o, sumbits);
}

__global__ __launch_bounds__(512, 2) void wattn_kernel(const uint16_t* __restrict__ x, const uint16_t* __restrict__ wqkv,
                                                       const uint16_t* __restrict__ wo, const uint16_t* __restrict__ rpb,
                                                       const int* __restrict__ rpidx, const uint16_t* __restrict__ mask,
                                                       uint16_t* __restrict__ out) {
    extern __shared__ __attribute__((aligned(16))) char smem_[];
    lds_ptr L = (lds_ptr)smem_;
    const uint32_t Lbase = (uint32_t)(uintptr_t)L;
    const int tid = threadIdx.x, lane = tid & 63;
    const int wv = __builtin_amdgcn_readfirstlane(tid >> 6);
    const int h = wv & 3, p = wv >> 2;
    const int g = lane >> 4, m = lane & 15;
    const int blk = blockIdx.x;
    const int wh = blk >> 3, ww0 = (blk & 7) * 4;

    u32x4 wreg[9];
    int wrow[3], wj[3];
    if (wv < 6) {
#pragma unroll
        for (int i = 0; i < 3; ++i) {
            int c = 64 * i + lane;
            int r = c / 12;
            wj[i] = c - 12 * r;
            wrow[i] = 16 * wv + r;
        }
#pragma unroll
        for (int part = 0; part < 3; ++part)
#pragma unroll
            for (int i = 0; i < 3; ++i)
                wreg[part * 3 + i] = *(const u32x4*)((const char*)wqkv + wrow[i] * 576 + part * 192 + wj[i] * 16);
    }
    const int bA = lane / 12, bB = (64 + lane) / 12;
    auto load_x = [&](int i) {
        const int ww = ww0 + i;
        const int x0 = 7 * ww + 3;
        const int wrapA = (ww == 31 && bA >= 4) ? -43008 : 0;
        const int wrapB = (ww == 31) ? -43008 : 0;
        for (int a = h; a < 7; a += 4) {
            int y = 7 * wh + a + 3;
            if (y >= 224) y -= 224;
            const int rowbase = (y * 224 + x0) * 192;
            const int tA = 7 * a + bA, tB = 7 * a + bB;
            const uint32_t offA = (uint32_t)(rowbase + wrapA + ((16 * lane) ^ ((tA << 2) & 32)));
            const uint32_t offB = (uint32_t)(rowbase + wrapB + ((1024 + 16 * lane) ^ ((tB << 2) & 32)));
            const uint32_t dst = Lbase + OFF_X + i * XS + a * 1344;
            dma16(x, offA, dst);
            if (lane < 20) dma16(x, offB, dst + 1024);
        }
    };
    load_x(p);
    if (wv == 7) {
        int y = 7 * wh + 9; if (y >= 224) y -= 224;
        int xx = 7 * (ww0 + bA) + 9; if (xx >= 224) xx -= 224;
        if (lane < 48) dma16(x, (uint32_t)((y * 224 + xx) * 192 + 16 * (lane - 12 * bA)), Lbase + OFF_XL);
    }
    u32x4 idxv[3][3];
    int idx48[3], idxL;
#pragma unroll
    for (int qt = 0; qt < 3; ++qt) {
        const int q = 16 * qt + m;
#pragma unroll
        for (int kt = 0; kt < 3; ++kt) idxv[qt][kt] = *(const u32x4*)(rpidx + q * 49 + 16 * kt + 4 * g);
        idx48[qt] = rpidx[q * 49 + 48];
    }
    idxL = rpidx[48 * 49 + (lane < 48 ? lane : 48)];
    const int mc0 = 64 * h + lane, mc1 = 256 + 64 * h + lane;
    const int mq0 = mc0 / 7, mk0 = mc0 - 7 * mq0, mq1 = mc1 / 7, mk1 = mc1 - 7 * mq1;
    const int msrc0 = 98 * mq0 + (mk0 < 6 ? 16 * mk0 : 82), msrc1 = 98 * mq1 + (mk1 < 6 ? 16 * mk1 : 82);
    const int mdst0 = 128 * mq0 + 16 * (mk0 ^ ((mq0 >> 1) & 7)), mdst1 = 128 * mq1 + 16 * (mk1 ^ ((mq1 >> 1) & 7));
    u32x4 mreg0, mreg1 = {0, 0, 0, 0};
    auto load_mask = [&](int i) {
        const char* mb = (const char*)mask + (size_t)(4 * blk + i) * 4802;
        mreg0 = *(const u32x4*)(mb + msrc0);
        if (mc1 < 343) mreg1 = *(const u32x4*)(mb + msrc1);
    };
    auto store_mask = [&](int i) {
        lds_w128(L + OFF_MASK + i * MS + mdst0, mreg0);
        if (mc1 < 343) lds_w128(L + OFF_MASK + i * MS + mdst1, mreg1);
    };
    load_mask(p);
    uint32_t rpbv[6];
    if (wv >= 6) {
#pragma unroll
        for (int i = 0; i < 6; ++i) { const int e = tid - 384 + 128 * i; rpbv[i] = rpb[e < 676 ? e : 675]; }
    }

    if (wv >= 6) {
#pragma unroll
        for (int k = 0; k < 9; ++k) {
            const int kk = (wv - 6) * 9 + k;
            dma16(wo, kk * 1024 + lane * 16, Lbase + OFF_WORAW + kk * 1024);
        }
#pragma unroll
        for (int i = 0; i < 6; ++i) {
            const int e = tid - 384 + 128 * i;
            if (e < 676) *(LAS float*)(L + OFF_RPB + (e & 3) * 704 + (e >> 2) * 4) = bflo(rpbv[i]) * LOG2E;
        }
        if (wv == 6) lds_w32(L + OFF_ZERO + 4 * lane, 0u);
    } else {
#pragma unroll
        for (int i = 0; i < 3; ++i) {
            const int row = wrow[i];
            const int prow = row ^ (((row >> 3) & 1) << 2);
            const int hh = wj[i] / 3, piece = wj[i] - 3 * hh;
            lds_ptr dq = L + OFF_WIMG + (hh * 5 + piece) * TS + 32 * prow;
            u32x4 q = wreg[i], k = wreg[3 + i], v = wreg[6 + i];
            lds_w32(dq + 0, scale2(q[0]));
            lds_w32(dq + 8, scale2(q[1]));
            lds_w32(dq + 16, scale2(q[2]));
            lds_w32(dq + 24, scale2(q[3]));
            lds_w32(dq + 4, k[0]);
            lds_w32(dq + 12, k[1]);
            lds_w32(dq + 20, k[2]);
            lds_w32(dq + 28, k[3]);
            if (piece < 2) {
                lds_w128(L + OFF_WIMG + (hh * 5 + 3) * TS + 32 * prow + 16 * piece, v);
            } else {
                lds_ptr dv = L + OFF_WIMG + (hh * 5 + 4) * TS + 32 * prow;
                u32x4 a = {v[0], 0u, v[1], 0u}, b = {v[2], 0u, v[3], 0u};
                lds_w128(dv, a);
                lds_w128(dv + 16, b);
            }
        }
    }
    store_mask(p);
    WAIT_VM(0);
    BARRIER();

    load_x(p + 2);
    load_mask(p + 2);

    u32x4 Wf[5][3];
    {
        const int q4 = m >> 2, p4 = m & 3;
        lds_ptr a1 = L + OFF_WIMG + h * 5 * TS + 32 * (8 * g + q4 + 4 * (g & 1)) + 8 * p4;
        lds_ptr a2 = L + OFF_WIMG + h * 5 * TS + 32 * (8 * g + q4 + 4 * (1 - (g & 1))) + 8 * p4;
#pragma unroll
        for (int t = 0; t < 5; ++t)
#pragma unroll
            for (int s = 0; s < 3; ++s) {
                u32x2 lo = lds_tr(a1 + t * TS + 1024 * s), hi = lds_tr(a2 + t * TS + 1024 * s);
                Wf[t][s] = u32x4{lo[0], lo[1], hi[0], hi[1]};
            }
    }
    const float onec = ((m & 3) >= 2) ? 1.0f : 0.0f;
    const f32x4 ONES = {onec, onec, onec, onec};
    const f32x4 ZERO = {0.f, 0.f, 0.f, 0.f};
    auto project = [&](lds_ptr xa, u32x4& Q, u32x4& K, u32x2& V0, u32x2& V1) {
        u32x4 xf0 = lds_r128(xa), xf1 = lds_r128(xa + 64), xf2 = lds_r128(xa + 128);
        f32x4 a0 = mfma(Wf[0][0], xf0, ZERO), a1 = mfma(Wf[1][0], xf0, ZERO), a2 = mfma(Wf[2][0], xf0, ZERO);
        f32x4 v0 = mfma(xf0, Wf[3][0], ZERO), v1 = mfma(xf0, Wf[4][0], ONES);
        a0 = mfma(Wf[0][1], xf1, a0); a1 = mfma(Wf[1][1], xf1, a1); a2 = mfma(Wf[2][1], xf1, a2);
        v0 = mfma(xf1, Wf[3][1], v0); v1 = mfma(xf1, Wf[4][1], v1);
        a0 = mfma(Wf[0][2], xf2, a0); a1 = mfma(Wf[1][2], xf2, a1); a2 = mfma(Wf[2][2], xf2, a2);
        v0 = mfma(xf2, Wf[3][2], v0); v1 = mfma(xf2, Wf[4][2], v1);
        Q = u32x4{pk(a0[0], a0[1]), pk(a1[0], a1[1]), pk(a2[0], a2[1]), 0u};
        K = u32x4{pk(a0[2], a0[3]), pk(a1[2], a1[3]), pk(a2[2], a2[3]), 0u};
        V0 = u32x2{pk(v0[0], v0[1]), pk(v0[2], v0[3])};
        V1 = u32x2{pk(v1[0], v1[1]), pk(v1[2], v1[3])};
    };
    u32x4 KL;
    u32x2 V0L, V1L;
    lds_ptr QL = L + OFF_QL + wv * 256;
    {
        u32x4 QLr;
        project(L + OFF_XL + 192 * (m >> 2) + 16 * g, QLr, KL, V0L, V1L);
        if ((m & 3) == 0) lds_w128(QL + (m >> 2) * 64 + g * 16, QLr);
    }
    f32x4 bias[3][3];
    float bias48[3], biasL;
#pragma unroll
    for (int qt = 0; qt < 3; ++qt) {
#pragma unroll
        for (int kt = 0; kt < 3; ++kt)
#pragma unroll
            for (int r = 0; r < 4; ++r) bias[qt][kt][r] = *(const LAS float*)(L + OFF_RPB + 4 * idxv[qt][kt][r] + 704 * h);
        bias48[qt] = *(const LAS float*)(L + OFF_RPB + 4 * idx48[qt] + 704 * h);
    }
    biasL = *(const LAS float*)(L + OFF_RPB + 4 * idxL + 704 * h);
    BARRIER();
    {
        const int q4 = m >> 2, p4 = m & 3;
        for (int f = wv; f < 18; f += 8) {
            const int t = f / 3, s = f - 3 * t;
            const int d = (s == 0) ? 4 * g + (q4 & 1) : (s == 1) ? 4 * g + 2 + (q4 & 1) : 16 + 2 * g + (q4 & 1);
            const int r1 = 24 * (((q4 >> 1)) ^ (g & 1)) + d;
            const int r2 = 24 * ((2 + (q4 >> 1)) ^ (g & 1)) + d;
            const int col = (32 * (t >> 1) + 8 * p4 + 4 * (t & 1)) * 2;
            u32x2 lo = lds_tr(L + OFF_WORAW + r1 * 192 + col), hi = lds_tr(L + OFF_WORAW + r2 * 192 + col);
            lds_w128(L + OFF_WOFRAG + f * 1024 + lane * 16, u32x4{lo[0], lo[1], hi[0], hi[1]});
        }
    }
    u32x4 IHL;
    {
        const uint32_t val = (g < 2) ? 0x3FB9u : 0xBB2Cu;
        const int j = m - 8 * (g & 1);
#pragma unroll
        for (int r = 0; r < 4; ++r) IHL[r] = (j == 2 * r) ? val : (j == 2 * r + 1) ? (val << 16) : 0u;
    }
    const int xsw = (16 * g) ^ (((m >> 3) & 1) << 5);
    const int vsw = (m >> 1) & 7;
    const int ma0 = 128 * m + 16 * ((0 + (g & 1)) ^ vsw), ma1 = 128 * m + 16 * ((2 + (g & 1)) ^ vsw), ma2 = 128 * m + 16 * ((4 + (g & 1)) ^ vsw);
    const int m48a = 128 * m + 16 * (6 ^ vsw) + 14;
    const int mLa = 128 * 48 + (lane < 48 ? 2 * lane : 110);
    int qa[4];
#pragma unroll
    for (int kt = 0; kt < 4; ++kt) qa[kt] = ((m >> 2) == kt) ? 16 * g : (OFF_ZERO - OFF_QL - wv * 256 + 16 * g);
    lds_ptr PL = L + OFF_PL + wv * 128;
    const int role = (h + 2 * p) & 3;

    for (int it = 0; it < 2; ++it) {
        const int i = p + 2 * it;
        const int ww = ww0 + i;
        lds_ptr X = L + OFF_X + i * XS;
        lds_ptr M = L + OFF_MASK + i * MS;
        lds_ptr OB = L + OFF_OB + i * OBS;
        lds_ptr OL = L + OFF_OL + p * 3072;
        Frags F;
        {
            u32x2 V0[3], V1[3];
#pragma unroll
            for (int tt = 0; tt < 3; ++tt) project(X + 192 * (16 * tt + m) + xsw, F.Q[tt], F.K[tt], V0[tt], V1[tt]);
            F.K[3] = KL;
            F.VT[0][0] = u32x4{V0[0][0], V0[0][1], V0[1][0], V0[1][1]};
            F.VT[0][1] = u32x4{V0[2][0], V0[2][1], V0L[0], V0L[1]};
            F.VT[1][0] = u32x4{V1[0][0], V1[0][1], V1[1][0], V1[1][1]};
            F.VT[1][1] = u32x4{V1[2][0], V1[2][1], V1L[0], V1L[1]};
        }
        uint32_t o[4][3], sb[4];
        const bool gsel = (g == i);
        const bool validL = (lane < 48) || (lane == 48 + 4 * i);
        const float cL = validL ? __builtin_fmaf(bflo(lds_r16(M + mLa)), LOG2E, biasL) : NEG_BIG;
#pragma unroll
        for (int qt = 0; qt < 3; ++qt)
            attn_tile<false>(F, qt, bias[qt], bias48[qt], gsel, M + 2048 * qt, ma0, ma1, ma2, m48a, IHL, o[qt], sb[qt]);
        attn_left<false>(F, cL, validL, QL + 64 * i, qa, PL, o[3], sb[3]);
        {
            const uint32_t lo = min(min(sb[0], sb[1]), min(sb[2], sb[3])), hi = max(max(sb[0], sb[1]), max(sb[2], sb[3]));
            const bool bad = (lo < 0x2B800000u) || (hi > 0x53800000u);
            if (__builtin_amdgcn_ballot_w64(bad) != 0ull) {
#pragma unroll
                for (int qt = 0; qt < 3; ++qt)
                    attn_tile<true>(F, qt, bias[qt], bias48[qt], gsel, M + 2048 * qt, ma0, ma1, ma2, m48a, IHL, o[qt], sb[qt]);
                attn_left<true>(F, cL, validL, QL + 64 * i, qa, PL, o[3], sb[3]);
            }
        }
        {
            const int osw = lane * 16 + 4 * (h ^ (g & 1));
#pragma unroll
            for (int qt = 0; qt < 3; ++qt)
#pragma unroll
                for (int s = 0; s < 3; ++s) lds_w32(OB + osw + qt * 3072 + s * 1024, o[qt][s]);
            if (m == it) {
#pragma unroll
                for (int s = 0; s < 3; ++s) lds_w32(OL + osw + s * 1024, o[3][s]);
            }
        }
        if (it == 0) { WAIT_VM(0); store_mask(p + 2); }
        BARRIER();
        if (role < 3 || it == 1) {
            lds_ptr orr = (role < 3 ? OB + role * 3072 : OL) + lane * 16;
            u32x4 of0 = lds_r128(orr), of1 = lds_r128(orr + 1024), of2 = lds_r128(orr + 2048);
            const int tok = (role < 3) ? 16 * role + m : 48;
            const int wwo = (role < 3) ? ww : ww0 + p + 2 * (m & 1);
            const int a = tok / 7, b = tok - 7 * a;
            int y = 7 * wh + a + 3; if (y >= 224) y -= 224;
            int xx = 7 * wwo + b + 3; if (xx >= 224) xx -= 224;
            char* dst = (char*)out + (y * 224 + xx) * 192 + 16 * g;
            lds_ptr wf = L + OFF_WOFRAG + lane * 16;
#pragma unroll
            for (int i2 = 0; i2 < 3; ++i2) {
                f32x4 c0 = mfma(lds_r128(wf + (6 * i2 + 0) * 1024), of0, ZERO);
                f32x4 c1 = mfma(lds_r128(wf + (6 * i2 + 3) * 1024), of0, ZERO);
                c0 = mfma(lds_r128(wf + (6 * i2 + 1) * 1024), of1, c0);
                c1 = mfma(lds_r128(wf + (6 * i2 + 4) * 1024), of1, c1);
                c0 = mfma(lds_r128(wf + (6 * i2 + 2) * 1024), of2, c0);
                c1 = mfma(lds_r128(wf + (6 * i2 + 5) * 1024), of2, c1);
                u32x4 r = {pk(c0[0], c0[1]), pk(c0[2], c0[3]), pk(c1[0], c1[1]), pk(c1[2], c1[3])};
                if (role < 3 || m < 2) *(u32x4*)(dst + 64 * i2) = r;
            }
        }
    }
}

extern "C" void kernel_launch(void* const* d_in, const int* in_sizes, int n_in, void* d_out, int out_size, void* d_ws,
                              size_t ws_size, hipStream_t stream) {
    (void)in_sizes; (void)n_in; (void)out_size; (void)d_ws; (void)ws_size;
    (void)hipFuncSetAttribute((const void*)wattn_kernel, hipFuncAttributeMaxDynamicSharedMemorySize, LDS_TOTAL);
    wattn_kernel<<<256, 512, LDS_TOTAL, stream>>>((const uint16_t*)d_in[0], (const uint16_t*)d_in[1], (const uint16_t*)d_in[2],
                                                  (const uint16_t*)d_in[3], (const int*)d_in[4], (const uint16_t*)d_in[5],
                                                  (uint16_t*)d_out);
}
```

```cpp
#include <hip/hip_runtime.h>
#include <hip/hip_bf16.h>
#include <stdint.h>
#include <type_traits>

typedef __attribute__((ext_vector_type(8))) __bf16 bf16x8;
typedef __attribute__((ext_vector_type(2))) __bf16 bf16x2;
typedef __attribute__((ext_vector_type(4))) float f32x4;
typedef __attribute__((ext_vector_type(2))) float f32x2;
typedef __attribute__((ext_vector_type(4))) uint32_t u32x4;
typedef __attribute__((ext_vector_type(2))) uint32_t u32x2;
typedef __attribute__((ext_vector_type(4))) short s16x4;

#define DEV __device__ __forceinline__
#define LAS __attribute__((address_space(3)))
typedef LAS char* lds_ptr;

constexpr int OFF_X = 0, XS = 9408;
constexpr int OFF_MASK = 37632, MS = 6272;
constexpr int OFF_W = 62720, WP = 608;
constexpr int OFF_IDX = 121088;
constexpr int OFF_RPB = 131280;
constexpr int OFF_XL = 134096;
constexpr int OFF_QL = 134864;
constexpr int OFF_ZERO = 136912;
constexpr int OFF_PL = 137168;
constexpr int OFF_OL = 138192;
constexpr int OFF_CNT = 144336;
constexpr int OFF_WORAW = 144400;
constexpr int LDS_TOTAL = 144400 + 18432;
constexpr int OFF_OB = OFF_W;
constexpr int OBS = 9216;

constexpr float LOG2E = 1.4426950408889634f;
constexpr float QSCALE = 0.20412414523193154f * 1.4426950408889634f;
constexpr float NEG_BIG = -1.0e30f;

DEV uint32_t pk(float a, float b) {
    f32x2 v = {a, b};
    bf16x2 r = __builtin_convertvector(v, bf16x2);
    return __builtin_bit_cast(uint32_t, r);
}
DEV f32x4 mfma(u32x4 a, u32x4 b, f32x4 c) {
    return __builtin_amdgcn_mfma_f32_16x16x32_bf16(__builtin_bit_cast(bf16x8, a), __builtin_bit_cast(bf16x8, b), c, 0, 0, 0);
}
DEV u32x4 lds_r128(lds_ptr p) { return *(const LAS u32x4*)p; }
DEV u32x2 lds_r64(lds_ptr p) { return *(const LAS u32x2*)p; }
DEV uint32_t lds_r16(lds_ptr p) { return (uint32_t) * (const LAS uint16_t*)p; }
DEV void lds_w128(lds_ptr p, u32x4 v) { *(LAS u32x4*)p = v; }
DEV void lds_w32(lds_ptr p, uint32_t v) { *(LAS uint32_t*)p = v; }
DEV u32x2 lds_tr(lds_ptr p) {
    return __builtin_bit_cast(u32x2, __builtin_amdgcn_ds_read_tr16_b64_v4i16((LAS s16x4*)p));
}
DEV float bflo(uint32_t v) { return __builtin_bit_cast(float, v << 16); }
DEV float bfhi(uint32_t v) { return __builtin_bit_cast(float, v & 0xffff0000u); }
DEV float ex2(float v) { return __builtin_amdgcn_exp2f(v); }

DEV void dma16(const void* sbase, uint32_t voff, uint32_t lds_dst) {
    unsigned keep;
    asm volatile("s_mov_b32 %0, m0\n\ts_mov_b32 m0, %3\n\ts_nop 0\n\tglobal_load_lds_dwordx4 %1, %2\n\ts_mov_b32 m0, %0"
                 : "=&s"(keep) : "v"(voff), "s"(sbase), "s"(lds_dst) : "memory");
}
DEV void dma16nt(const void* sbase, uint32_t voff, uint32_t lds_dst) {
    unsigned keep;
    asm volatile("s_mov_b32 %0, m0\n\ts_mov_b32 m0, %3\n\ts_nop 0\n\tglobal_load_lds_dwordx4 %1, %2 nt\n\ts_mov_b32 m0, %0"
                 : "=&s"(keep) : "v"(voff), "s"(sbase), "s"(lds_dst) : "memory");
}
DEV uint32_t gload32(const void* sbase, uint32_t voff) {
    uint32_t v;
    asm volatile("global_load_dword %0, %1, %2" : "=v"(v) : "v"(voff), "s"(sbase) : "memory");
    return v;
}
#define BARRIER() do { asm volatile("s_waitcnt lgkmcnt(0)" ::: "memory"); __builtin_amdgcn_s_barrier(); asm volatile("" ::: "memory"); } while (0)
#define WAIT_VM(n) asm volatile("s_waitcnt vmcnt(" #n ")" ::: "memory")

DEV void stream_sync(lds_ptr cnt, uint32_t target) {
    asm volatile("s_waitcnt lgkmcnt(0)" ::: "memory");
    if ((threadIdx.x & 63) == 0) __hip_atomic_fetch_add((LAS uint32_t*)cnt, 1u, __ATOMIC_RELAXED, __HIP_MEMORY_SCOPE_WORKGROUP);
    while (true) {
        uint32_t v = __hip_atomic_load((LAS uint32_t*)cnt, __ATOMIC_RELAXED, __HIP_MEMORY_SCOPE_WORKGROUP);
        if (__builtin_amdgcn_readfirstlane(v) >= target) break;
        __builtin_amdgcn_s_sleep(1);
    }
    asm volatile("" ::: "memory");
}

struct Frags {
    u32x4 Q[3], K[4];
    u32x4 VT[2][2];
};

DEV void pv_norm(const Frags& F, u32x4 P0, u32x4 P1, uint32_t (&o)[3], uint32_t& sumbits) {
    f32x4 zero = {0.f, 0.f, 0.f, 0.f};
    f32x4 oa = mfma(F.VT[0][0], P0, zero);
    f32x4 ob = mfma(F.VT[1][0], P0, zero);
    oa = mfma(F.VT[0][1], P1, oa);
    ob = mfma(F.VT[1][1], P1, ob);
    float sum = ob[2];
    sumbits = __builtin_bit_cast(uint32_t, sum);
    float rs = __builtin_amdgcn_rcpf(sum);
    o[0] = pk(oa[0] * rs, oa[1] * rs);
    o[1] = pk(oa[2] * rs, oa[3] * rs);
    o[2] = pk(ob[0] * rs, ob[1] * rs);
}

template <bool SAFE>
DEV void attn_tile(const Frags& F, u32x4 Q, const f32x4 (&bias)[3], f32x4 CW, lds_ptr M, int ma0, int ma1, int ma2,
                   u32x4 IHL, uint32_t (&o)[3], uint32_t& sumbits) {
    f32x4 s0, s1, s2;
    float s30;
    if (!SAFE) {
        s0 = mfma(F.K[0], Q, bias[0]);
        s1 = mfma(F.K[1], Q, bias[1]);
        s2 = mfma(F.K[2], Q, bias[2]);
        f32x4 s3 = mfma(F.K[3], Q, CW);
        s30 = s3[0];
        u32x4 m0 = lds_r128(M + ma0), m1 = lds_r128(M + ma1), m2 = lds_r128(M + ma2);
        s0 = mfma(IHL, m0, s0);
        s1 = mfma(IHL, m1, s1);
        s2 = mfma(IHL, m2, s2);
    } else {
        const int lane = threadIdx.x & 63;
        const int g = lane >> 4, mm = lane & 15;
        const int v = (mm >> 1) & 7;
        const u32x4 Qz = Q;
        f32x4 zero = {0.f, 0.f, 0.f, 0.f};
        s0 = mfma(F.K[0], Qz, bias[0]);
        s1 = mfma(F.K[1], Qz, bias[1]);
        s2 = mfma(F.K[2], Qz, bias[2]);
        f32x4 s3 = mfma(F.K[3], Qz, zero);
        const int h4 = 4 * (__builtin_amdgcn_readfirstlane(threadIdx.x >> 6) & 3);
        const uint32_t bb = *(const LAS uint32_t*)(M + 128 * mm + 16 * (7 ^ v) + h4);
        const float m48 = bflo(lds_r16(M + 128 * mm + 16 * (6 ^ v) + 14));
        s30 = __builtin_fmaf(m48, LOG2E, s3[0] + (bflo(bb) + bfhi(bb)));
        u32x2 mv0 = lds_r64(M + 128 * mm + 16 * ((0 + (g >> 1)) ^ v) + 8 * (g & 1));
        u32x2 mv1 = lds_r64(M + 128 * mm + 16 * ((2 + (g >> 1)) ^ v) + 8 * (g & 1));
        u32x2 mv2 = lds_r64(M + 128 * mm + 16 * ((4 + (g >> 1)) ^ v) + 8 * (g & 1));
#define ADDM(s, mv) s[0] = __builtin_fmaf(bflo(mv[0]), LOG2E, s[0]); s[1] = __builtin_fmaf(bfhi(mv[0]), LOG2E, s[1]); \
                    s[2] = __builtin_fmaf(bflo(mv[1]), LOG2E, s[2]); s[3] = __builtin_fmaf(bfhi(mv[1]), LOG2E, s[3]);
        ADDM(s0, mv0) ADDM(s1, mv1) ADDM(s2, mv2)
#undef ADDM
        s30 = (CW[0] == 0.f) ? s30 : -INFINITY;
        float mx = s30;
#pragma unroll
        for (int r = 0; r < 4; ++r) mx = fmaxf(mx, fmaxf(s0[r], fmaxf(s1[r], s2[r])));
        mx = fmaxf(mx, __shfl_xor(mx, 16));
        mx = fmaxf(mx, __shfl_xor(mx, 32));
#pragma unroll
        for (int r = 0; r < 4; ++r) { s0[r] -= mx; s1[r] -= mx; s2[r] -= mx; }
        s30 -= mx;
    }
    u32x4 P0 = {pk(ex2(s0[0]), ex2(s0[1])), pk(ex2(s0[2]), ex2(s0[3])), pk(ex2(s1[0]), ex2(s1[1])), pk(ex2(s1[2]), ex2(s1[3]))};
    u32x4 P1 = {pk(ex2(s2[0]), ex2(s2[1])), pk(ex2(s2[2]), ex2(s2[3])), pk(ex2(s30), 0.f), 0u};
    pv_norm(F, P0, P1, o, sumbits);
}

template <bool SAFE>
DEV void attn_left(const Frags& F, float cinit, bool valid, lds_ptr QLw, const int (&qa)[4], lds_ptr PL, uint32_t (&o)[3],
                   uint32_t& sumbits) {
    const int lane = threadIdx.x & 63;
    const int g = lane >> 4;
    f32x4 s = {cinit, 0.f, 0.f, 0.f};
#pragma unroll
    for (int kt = 0; kt < 4; ++kt) s = mfma(lds_r128(QLw + qa[kt]), F.K[kt], s);
    float v = s[0];
    if (SAFE) {
        v = valid ? v : -INFINITY;
        float mx = v;
#pragma unroll
        for (int d = 1; d < 64; d <<= 1) mx = fmaxf(mx, __shfl_xor(mx, d));
        v -= mx;
    }
    *(LAS uint16_t*)(PL + 2 * lane) = (uint16_t)pk(ex2(v), 0.f);
    u32x2 a0 = lds_r64(PL + 8 * g), a1 = lds_r64(PL + 8 * g + 32), a2 = lds_r64(PL + 8 * g + 64), a3 = lds_r64(PL + 8 * g + 96);
    u32x4 P0 = {a0[0], a0[1], a1[0], a1[1]}, P1 = {a2[0], a2[1], a3[0], a3[1]};
    pv_norm(F, P0, P1, o, sumbits);
}

__global__ __launch_bounds__(512, 2) void wattn_kernel(const uint16_t* __restrict__ x, const uint16_t* __restrict__ wqkv,
                                                       const uint16_t* __restrict__ wo, const uint16_t* __restrict__ rpb,
                                                       const int* __restrict__ rpidx, const uint16_t* __restrict__ mask,
                                                       uint16_t* __restrict__ out) {
    extern __shared__ __attribute__((aligned(16))) char smem_[];
    lds_ptr L = (lds_ptr)smem_;
    const uint32_t Lbase = (uint32_t)(uintptr_t)L;
    const int tid = threadIdx.x, lane = tid & 63;
    const int wv = __builtin_amdgcn_readfirstlane(tid >> 6);
    const int h = wv & 3, p = wv >> 2;
    const int g = lane >> 4, m = lane & 15;
    const int blk = blockIdx.x;
    const int wh = blk >> 3, ww0 = (blk & 7) * 4;

    const uint32_t rsrc = 4 * (tid < 338 ? tid : 337);
    uint32_t isrc[2];
    bool iok[2];
#pragma unroll
    for (int j = 0; j < 2; ++j) {
        const int pos = 80 * wv + 64 * j + lane;
        const int q = pos / 13, c = pos - 13 * q;
        isrc[j] = (uint32_t)(196 * q + (c < 12 ? 16 * c : 180));
        iok[j] = (j == 0 || lane < 16) && pos < 637;
    }
    int xrel[3], xwr[3];
#pragma unroll
    for (int j = 0; j < 3; ++j) {
        const int pw = 64 * j + lane;
        const int pos = 147 * h + (pw < 147 ? pw : 146);
        const int t = pos / 12, c = pos - 12 * t;
        const int a = t / 7, b = t - 7 * a;
        xrel[j] = (a * 224 + b) * 192 + ((16 * c) ^ ((t << 2) & 32)) + ((wh == 31 && a >= 4) ? -9633792 : 0);
        xwr[j] = (b >= 4) ? -43008 : 0;
    }
    uint32_t wsrc[3];
    bool wok[3];
#pragma unroll
    for (int j = 0; j < 3; ++j) {
        const int sl = 64 * j + lane;
        const int r4 = sl / 38, c = sl - 38 * r4;
        wsrc[j] = 576 * r4 + 16 * c;
        wok[j] = c < 36 && sl < 152;
    }
    uint32_t msrc[2];
    bool mok[2];
#pragma unroll
    for (int j = 0; j < 2; ++j) {
        const int pos = 98 * h + 64 * j + lane;
        const int q = pos >> 3, mk = (pos & 7) ^ ((q >> 1) & 7);
        msrc[j] = 98 * q + (mk < 6 ? 16 * mk : 82);
        mok[j] = (mk < 7) && (j == 0 || lane < 34);
    }
    asm volatile("" :: "v"(isrc[0]), "v"(isrc[1]), "v"(xrel[0]), "v"(xrel[1]), "v"(xrel[2]), "v"(xwr[0]), "v"(xwr[1]), "v"(xwr[2]));
    asm volatile("" :: "v"(wsrc[0]), "v"(wsrc[1]), "v"(wsrc[2]), "v"(msrc[0]), "v"(msrc[1]), "v"(rsrc));
    auto load_x = [&](int i) {
        const int ww = ww0 + i;
        const int org = ((7 * wh + 3) * 224 + 7 * ww + 3) * 192;
        const uint32_t dst = Lbase + OFF_X + i * XS + 147 * 16 * h;
        const bool wr = ww == 31;
        dma16nt(x, (uint32_t)(org + xrel[0] + (wr ? xwr[0] : 0)), dst);
        dma16nt(x, (uint32_t)(org + xrel[1] + (wr ? xwr[1] : 0)), dst + 1024);
        if (lane < 19) dma16nt(x, (uint32_t)(org + xrel[2] + (wr ? xwr[2] : 0)), dst + 2048);
    };
    auto load_w = [&](int s) {
        const int kb = 4 * (wv ^ ((wv >> 1) & 1));
        const char* src = (const char*)wqkv + (32 * s + kb) * 576;
#pragma unroll
        for (int j = 0; j < 3; ++j)
            if (wok[j]) dma16(src, wsrc[j], Lbase + OFF_W + (32 * s + 4 * wv) * WP + 1024 * j);
    };
    auto load_mask = [&](int i) {
        const char* mb = (const char*)mask + (size_t)(4 * blk + i) * 4802;
        const uint32_t dst = Lbase + OFF_MASK + i * MS + 98 * 16 * h;
        if (mok[0]) dma16nt(mb, msrc[0], dst);
        if (mok[1]) dma16nt(mb, msrc[1], dst + 1024);
    };
    auto load_wo = [&]() {
        const uint32_t o = 2304 * wv + 16 * lane;
        dma16(wo, o, Lbase + OFF_WORAW + 2304 * wv);
        dma16(wo, o + 1024, Lbase + OFF_WORAW + 2304 * wv + 1024);
        if (lane < 16) dma16(wo, o + 2048, Lbase + OFF_WORAW + 2304 * wv + 2048);
    };
    load_x(p);
    {
        int y = 7 * wh + 9; if (y >= 224) y -= 224;
        int xx = 7 * (ww0 + (wv >> 1)) + 9; if (xx >= 224) xx -= 224;
        if (lane < 6) dma16nt(x, (uint32_t)((y * 224 + xx) * 192 + 96 * (wv & 1) + 16 * lane), Lbase + OFF_XL + 96 * wv);
    }
    uint32_t rpbv = gload32(rpb, rsrc);
    if (iok[0]) dma16(rpidx, isrc[0], Lbase + OFF_IDX + 16 * (80 * wv));
    if (iok[1]) dma16(rpidx, isrc[1], Lbase + OFF_IDX + 16 * (80 * wv + 64));
    load_w(0);
    load_w(1);
    if (tid < 192) lds_w128(L + OFF_W + (tid >> 1) * WP + 576 + 16 * (tid & 1), u32x4{0u, 0u, 0u, 0u});
    if (wv == 6) lds_w32(L + OFF_ZERO + 4 * lane, 0u);
    if (wv == 7 && lane < 2) lds_w32(L + OFF_CNT + 4 * lane, 0u);

    const float onec = ((m & 3) >= 2) ? 1.0f : 0.0f;
    const f32x4 ONES = {onec, onec, onec, onec};
    const f32x4 ZERO = {0.f, 0.f, 0.f, 0.f};
    const float qsc = (g & 1) ? 1.0f : QSCALE;
    const int xsw = (16 * g) ^ (((m >> 3) & 1) << 5);
    lds_ptr QL = L + OFF_QL + wv * 256;
    lds_ptr PL = L + OFF_PL + wv * 128;
    const int role = (h + 2 * p) & 3;
    lds_ptr CNT = L + OFF_CNT + 4 * p;
    int wa1, wa2, wc, wv1;
    {
        const int q4 = m >> 2, p4 = m & 3;
        wa1 = OFF_W + WP * (8 * g + q4 + 4 * (g & 1));
        wa2 = OFF_W + WP * (8 * g + q4 + 4 * (1 - (g & 1)));
        wc = 48 * h + 32 + 8 * (p4 >> 1) + 192 * (p4 & 1);
        wv1 = (p4 < 2) ? 384 + 48 * h + 32 + 8 * p4 : 576 + 8 * (p4 - 2);
        wa1 += 0; wa2 += 0;
    }
    const int wab = 48 * h + 8 * (m & 3);
    const int bperm = 4 * (16 * g + (((m & 3) < 2) ? 2 * (m >> 2) + (m & 1) : 8));
    u32x4 Wf[5][3];
    auto load_wf = [&](int s) {
        const int so = 32 * WP * s;
        auto fr = [&](int off) -> u32x4 {
            u32x2 lo = lds_tr(L + wa1 + off + so), hi = lds_tr(L + wa2 + off + so);
            return u32x4{lo[0], lo[1], hi[0], hi[1]};
        };
        Wf[0][s] = fr(wab);
        Wf[1][s] = fr(wab + 192);
        Wf[2][s] = fr(wc);
        Wf[3][s] = fr(wab + 384);
        const u32x4 t = fr(wv1);
#pragma unroll
        for (int r = 0; r < 4; ++r) Wf[4][s][r] = (uint32_t)__builtin_amdgcn_ds_bpermute(bperm, (int)t[r]);
    };
    struct Acc { f32x4 a0, a1, a2, v0, v1; };
    auto proj_step = [&](Acc& A, u32x4 xf, int s, bool first) {
        A.a0 = mfma(Wf[0][s], xf, first ? ZERO : A.a0);
        A.a1 = mfma(Wf[1][s], xf, first ? ZERO : A.a1);
        A.a2 = mfma(Wf[2][s], xf, first ? ZERO : A.a2);
        A.v0 = mfma(xf, Wf[3][s], first ? ZERO : A.v0);
        A.v1 = mfma(xf, Wf[4][s], first ? ONES : A.v1);
    };
    auto proj_fin = [&](const Acc& A, u32x4& Q, u32x4& K, u32x2& V0, u32x2& V1) {
        const uint32_t c0 = pk(A.a2[0] * qsc, A.a2[1] * qsc), c1 = pk(A.a2[2] * qsc, A.a2[3] * qsc);
        const auto sw = __builtin_amdgcn_permlane16_swap(c0, c1, false, false);
        Q[0] = pk(A.a0[0] * QSCALE, A.a0[1] * QSCALE); Q[1] = pk(A.a0[2] * QSCALE, A.a0[3] * QSCALE); Q[2] = sw[0];
        K[0] = pk(A.a1[0], A.a1[1]); K[1] = pk(A.a1[2], A.a1[3]); K[2] = sw[1];
        V0 = u32x2{pk(A.v0[0], A.v0[1]), pk(A.v0[2], A.v0[3])};
        V1 = u32x2{pk(A.v1[0], A.v1[1]), pk(A.v1[2], A.v1[3])};
    };
    auto project = [&](lds_ptr xa, u32x4& Q, u32x4& K, u32x2& V0, u32x2& V1) {
        u32x4 xf0 = lds_r128(xa), xf1 = lds_r128(xa + 64), xf2 = lds_r128(xa + 128);
        Acc A;
        proj_step(A, xf0, 0, true); proj_step(A, xf1, 1, false); proj_step(A, xf2, 2, false);
        proj_fin(A, Q, K, V0, V1);
    };
    Frags F;
    F.Q[0][3] = F.Q[1][3] = F.Q[2][3] = 0u;
    F.K[0][3] = F.K[1][3] = F.K[2][3] = 0u;
    F.K[3][3] = (g == 0) ? 0x3FB90000u : (g == 1) ? 0xBB2C0000u : (g == 2) ? 0x3F803F80u : 0u;
    u32x2 V0L, V1L;
    f32x4 bias[3][3];
    float biasL;
    u32x2 V0[3], V1[3];

    {
        lds_ptr X = L + OFF_X + p * XS;
        lds_ptr xl = L + OFF_XL + 192 * (m >> 2) + 16 * g;
        lds_ptr xa = X + 192 * m + xsw;
        Acc A[4];
        asm volatile("s_waitcnt vmcnt(8)" : "+v"(rpbv) :: "memory");
        if (tid < 338) {
            lds_ptr d = L + OFF_RPB + (tid & 1) * 1408 + (tid >> 1) * 4;
            *(LAS float*)d = bflo(rpbv) * LOG2E;
            *(LAS float*)(d + 704) = bfhi(rpbv) * LOG2E;
        }
        WAIT_VM(3);
        BARRIER();
        load_w(2);
        load_mask(p);
        load_wf(0);
        proj_step(A[3], lds_r128(xl), 0, true);
#pragma unroll
        for (int tt = 0; tt < 3; ++tt) proj_step(A[tt], lds_r128(xa + 3072 * tt), 0, true);
        {
            lds_ptr T = L + OFF_RPB + 704 * h;
#pragma unroll
            for (int qt = 0; qt < 3; ++qt)
#pragma unroll
                for (int kt = 0; kt < 3; ++kt) {
                    const u32x4 iv = lds_r128(L + OFF_IDX + 208 * (16 * qt + m) + 64 * kt + 16 * g);
#pragma unroll
                    for (int r = 0; r < 4; ++r) bias[qt][kt][r] = *(const LAS float*)(T + 4 * iv[r]);
                }
            const int l48 = lane < 48 ? lane : 48;
            const float b48 = *(const LAS float*)(T + 4 * *(const LAS uint32_t*)(L + OFF_IDX + 208 * l48 + 204));
            biasL = *(const LAS float*)(T + 4 * *(const LAS uint32_t*)(L + OFF_IDX + 208 * 48 + (lane < 48 ? 4 * lane : 204)));
            const uint32_t hi = pk(b48, 0.f) & 0xffffu;
            const uint32_t pr = hi | (pk(b48 - bflo(hi), 0.f) << 16);
            const int a48 = 128 * lane + 16 * (7 ^ ((lane >> 1) & 7)) + 4 * h;
            if (lane < 48) {
                lds_w32(L + OFF_MASK + p * MS + a48, pr);
                lds_w32(L + OFF_MASK + (p + 2) * MS + a48, pr);
            }
        }
        WAIT_VM(5);
        BARRIER();
        load_wo();
        load_x(p + 2);
        load_wf(1);
        proj_step(A[3], lds_r128(xl + 64), 1, false);
#pragma unroll
        for (int tt = 0; tt < 3; ++tt) proj_step(A[tt], lds_r128(xa + 3072 * tt + 64), 1, false);
        WAIT_VM(8);
        BARRIER();
        load_mask(p + 2);
        load_wf(2);
        proj_step(A[3], lds_r128(xl + 128), 2, false);
#pragma unroll
        for (int tt = 0; tt < 3; ++tt) proj_step(A[tt], lds_r128(xa + 3072 * tt + 128), 2, false);
        u32x4 QLr = {0u, 0u, 0u, 0u};
        proj_fin(A[3], QLr, F.K[3], V0L, V1L);
        if ((m & 3) == 0) lds_w128(QL + (m >> 2) * 64 + g * 16, QLr);
#pragma unroll
        for (int tt = 0; tt < 3; ++tt) proj_fin(A[tt], F.Q[tt], F.K[tt], V0[tt], V1[tt]);
        WAIT_VM(5);
        BARRIER();
    }

    int ln = lane;
    asm volatile("" : "+v"(ln));
    const int g2 = ln >> 4, m2 = ln & 15;
    u32x4 IHL;
    {
        const uint32_t val = (g2 < 2) ? 0x3FB9u : 0xBB2Cu;
        const int j = m2 - 8 * (g2 & 1);
#pragma unroll
        for (int r = 0; r < 4; ++r) IHL[r] = (j == 2 * r) ? val : (j == 2 * r + 1) ? (val << 16) : 0u;
    }
    const int vsw = (m2 >> 1) & 7;
    const int ma0 = 128 * m2 + 16 * ((0 + (g2 & 1)) ^ vsw), ma1 = 128 * m2 + 16 * ((2 + (g2 & 1)) ^ vsw), ma2 = 128 * m2 + 16 * ((4 + (g2 & 1)) ^ vsw);
    const int q3a = 128 * m2 + (g2 < 2 ? 16 * (6 ^ vsw) + 12 : 16 * (7 ^ vsw) + 4 * h);
    const int mLa = 128 * 48 + (ln < 48 ? 2 * ln : 110);
    int qa[4];
#pragma unroll
    for (int kt = 0; kt < 4; ++kt) qa[kt] = ((m2 >> 2) == kt) ? 16 * g : (OFF_ZERO - OFF_QL - wv * 256 + 16 * g2);
    int woa0, woa2;
    {
        const int q4 = m2 >> 2, p4 = m2 & 3;
        const int rb = 24 * ((q4 >> 1) ^ (g2 & 1)) + (q4 & 1);
        woa0 = OFF_WORAW + (rb + 4 * g2) * 192 + 16 * p4;
        woa2 = OFF_WORAW + (rb + 16 + 2 * g2) * 192 + 16 * p4;
    }
    auto wo_frag = [&](int colb, int s) -> u32x4 {
        lds_ptr a = L + (s == 2 ? woa2 : woa0 + (s == 1 ? 384 : 0)) + colb;
        u32x2 lo = lds_tr(a), hi = lds_tr(a + 48 * 192);
        return u32x4{lo[0], lo[1], hi[0], hi[1]};
    };

    auto iter = [&](auto it_tag) {
        constexpr int it = decltype(it_tag)::value;
        const int i = p + 2 * it;
        const int ww = ww0 + i;
        lds_ptr X = L + OFF_X + i * XS;
        lds_ptr M = L + OFF_MASK + i * MS;
        lds_ptr OB = L + OFF_OB + i * OBS;
        lds_ptr OL = L + OFF_OL + p * 3072;
        uint32_t o[4][3];
        const f32x4 CW = {(g == i) ? 0.f : NEG_BIG, 0.f, 0.f, 0.f};
        const bool validL = (lane < 48) || (lane == 48 + 4 * i);
        auto window = [&](auto safe_tag, auto skip_tag) -> bool {
            constexpr bool SAFE = decltype(safe_tag)::value;
            constexpr bool SKIP_PROJ = decltype(skip_tag)::value;
            if (!SKIP_PROJ) {
#pragma unroll
                for (int tt = 0; tt < 3; ++tt) project(X + 192 * (16 * tt + m) + xsw, F.Q[tt], F.K[tt], V0[tt], V1[tt]);
            }
            F.VT[0][0] = u32x4{V0[0][0], V0[0][1], V0[1][0], V0[1][1]};
            F.VT[0][1] = u32x4{V0[2][0], V0[2][1], V0L[0], V0L[1]};
            F.VT[1][0] = u32x4{V1[0][0], V1[0][1], V1[1][0], V1[1][1]};
            F.VT[1][1] = u32x4{V1[2][0], V1[2][1], V1L[0], V1L[1]};
#pragma unroll
            for (int tt = 0; tt < 3; ++tt) F.Q[tt][3] = SAFE ? 0u : *(const LAS uint32_t*)(M + 2048 * tt + q3a);
            uint32_t sb[4];
            const float mL = bflo(lds_r16(M + mLa));
            const float cL = validL ? __builtin_fmaf(mL, LOG2E, biasL) : NEG_BIG;
#pragma unroll
            for (int qt = 0; qt < 3; ++qt)
                attn_tile<SAFE>(F, F.Q[qt], bias[qt], CW, M + 2048 * qt, ma0, ma1, ma2, IHL, o[qt], sb[qt]);
            attn_left<SAFE>(F, cL, validL, QL + 64 * i, qa, PL, o[3], sb[3]);
            const uint32_t lo = min(min(sb[0], sb[1]), min(sb[2], sb[3])), hi = max(max(sb[0], sb[1]), max(sb[2], sb[3]));
            const bool bad = (lo < 0x2B800000u) || (hi > 0x53800000u);
            return __builtin_amdgcn_ballot_w64(bad) != 0ull;
        };
        if (window(std::false_type{}, std::integral_constant<bool, it == 0>{})) (void)window(std::true_type{}, std::false_type{});
        {
            const int osw = lane * 16 + 4 * (h ^ (g & 1));
#pragma unroll
            for (int qt = 0; qt < 3; ++qt)
#pragma unroll
                for (int s = 0; s < 3; ++s) lds_w32(OB + osw + qt * 3072 + s * 1024, o[qt][s]);
            if (m == it) {
#pragma unroll
                for (int s = 0; s < 3; ++s) lds_w32(OL + osw + s * 1024, o[3][s]);
            }
        }
        if (it == 0) WAIT_VM(0);
        stream_sync(CNT, 4 * (it + 1));
        int y0 = 7 * wh + 3;
        const int x0 = 7 * ww + 3;
        if (role < 3) {
            const int cb = 64 * role;
            u32x4 w0 = wo_frag(cb, 0), w1 = wo_frag(cb, 1), w2 = wo_frag(cb, 2);
            u32x4 w3 = wo_frag(cb + 8, 0), w4 = wo_frag(cb + 8, 1), w5 = wo_frag(cb + 8, 2);
#pragma unroll
            for (int qt = 0; qt < 3; ++qt) {
                lds_ptr orr = OB + qt * 3072 + lane * 16;
                u32x4 of0 = lds_r128(orr), of1 = lds_r128(orr + 1024), of2 = lds_r128(orr + 2048);
                const int tok = 16 * qt + m;
                const int a = (tok * 37) >> 8, b = tok - 7 * a;
                int y = y0 + a; if (y >= 224) y -= 224;
                int xx = x0 + b; if (xx >= 224) xx -= 224;
                f32x4 c0 = mfma(w0, of0, ZERO), c1 = mfma(w3, of0, ZERO);
                c0 = mfma(w1, of1, c0); c1 = mfma(w4, of1, c1);
                c0 = mfma(w2, of2, c0); c1 = mfma(w5, of2, c1);
                u32x4 r = {pk(c0[0], c0[1]), pk(c0[2], c0[3]), pk(c1[0], c1[1]), pk(c1[2], c1[3])};
                *(u32x4*)((char*)out + (y * 224 + xx) * 192 + 16 * g + 64 * role) = r;
            }
        } else if (it == 1) {
            lds_ptr orr = OL + lane * 16;
            u32x4 of0 = lds_r128(orr), of1 = lds_r128(orr + 1024), of2 = lds_r128(orr + 2048);
            int y = y0 + 6; if (y >= 224) y -= 224;
            int xx = 7 * (ww0 + p + 2 * (m & 1)) + 9; if (xx >= 224) xx -= 224;
            char* dst = (char*)out + (y * 224 + xx) * 192 + 16 * g;
#pragma unroll
            for (int i2 = 0; i2 < 3; ++i2) {
                f32x4 c0 = mfma(wo_frag(64 * i2, 0), of0, ZERO);
                f32x4 c1 = mfma(wo_frag(64 * i2 + 8, 0), of0, ZERO);
                c0 = mfma(wo_frag(64 * i2, 1), of1, c0);
                c1 = mfma(wo_frag(64 * i2 + 8, 1), of1, c1);
                c0 = mfma(wo_frag(64 * i2, 2), of2, c0);
                c1 = mfma(wo_frag(64 * i2 + 8, 2), of2, c1);
                u32x4 r = {pk(c0[0], c0[1]), pk(c0[2], c0[3]), pk(c1[0], c1[1]), pk(c1[2], c1[3])};
                if (m < 2) *(u32x4*)(dst + 64 * i2) = r;
            }
        }
    };
    iter(std::integral_constant<int, 0>{});
    iter(std::integral_constant<int, 1>{});
}

extern "C" void kernel_launch(void* const* d_in, const int* in_sizes, int n_in, void* d_out, int out_size, void* d_ws,
                              size_t ws_size, hipStream_t stream) {
    (void)in_sizes; (void)n_in; (void)out_size; (void)d_ws; (void)ws_size;
    (void)hipFuncSetAttribute((const void*)wattn_kernel, hipFuncAttributeMaxDynamicSharedMemorySize, LDS_TOTAL);
    wattn_kernel<<<256, 512, LDS_TOTAL, stream>>>((const uint16_t*)d_in[0], (const uint16_t*)d_in[1], (const uint16_t*)d_in[2],
                                                  (const uint16_t*)d_in[3], (const int*)d_in[4], (const uint16_t*)d_in[5],
                                                  (uint16_t*)d_out);
}
```

```cpp
#include <hip/hip_runtime.h>
#include <hip/hip_bf16.h>
#include <stdint.h>
#include <type_traits>

typedef __attribute__((ext_vector_type(8))) __bf16 bf16x8;
typedef __attribute__((ext_vector_type(2))) __bf16 bf16x2;
typedef __attribute__((ext_vector_type(4))) float f32x4;
typedef __attribute__((ext_vector_type(2))) float f32x2;
typedef __attribute__((ext_vector_type(4))) uint32_t u32x4;
typedef __attribute__((ext_vector_type(2))) uint32_t u32x2;
typedef __attribute__((ext_vector_type(4))) short s16x4;

#define DEV __device__ __forceinline__
#define LAS __attribute__((address_space(3)))
typedef LAS char* lds_ptr;

constexpr int OFF_X = 0, XS = 9408;
constexpr int OFF_MASK = 37632, MS = 6272;
constexpr int OFF_W = 62720, WP = 608;
constexpr int OFF_IDX = 121088;
constexpr int OFF_RPB = 131280;
constexpr int OFF_XL = 134096;
constexpr int OFF_QL = 134864;
constexpr int OFF_ZERO = 136912;
constexpr int OFF_PL = 137168;
constexpr int OFF_OL = 138192;
constexpr int OFF_CNT = 144336;
constexpr int OFF_WORAW = 144400;
constexpr int LDS_TOTAL = 144400 + 18432;
constexpr int OFF_OB = OFF_W;
constexpr int OBS = 9216;

constexpr float LOG2E = 1.4426950408889634f;
constexpr float QSCALE = 0.20412414523193154f * 1.4426950408889634f;
constexpr float NEG_BIG = -1.0e30f;

DEV uint32_t pk(float a, float b) {
    f32x2 v = {a, b};
    bf16x2 r = __builtin_convertvector(v, bf16x2);
    return __builtin_bit_cast(uint32_t, r);
}
DEV f32x4 mfma(u32x4 a, u32x4 b, f32x4 c) {
    return __builtin_amdgcn_mfma_f32_16x16x32_bf16(__builtin_bit_cast(bf16x8, a), __builtin_bit_cast(bf16x8, b), c, 0, 0, 0);
}
DEV u32x4 lds_r128(lds_ptr p) { return *(const LAS u32x4*)p; }
DEV u32x2 lds_r64(lds_ptr p) { return *(const LAS u32x2*)p; }
DEV uint32_t lds_r16(lds_ptr p) { return (uint32_t) * (const LAS uint16_t*)p; }
DEV void lds_w128(lds_ptr p, u32x4 v) { *(LAS u32x4*)p = v; }
DEV void lds_w32(lds_ptr p, uint32_t v) { *(LAS uint32_t*)p = v; }
DEV u32x2 lds_tr(lds_ptr p) {
    return __builtin_bit_cast(u32x2, __builtin_amdgcn_ds_read_tr16_b64_v4i16((LAS s16x4*)p));
}
DEV float bflo(uint32_t v) { return __builtin_bit_cast(float, v << 16); }
DEV float bfhi(uint32_t v) { return __builtin_bit_cast(float, v & 0xffff0000u); }
DEV float ex2(float v) { return __builtin_amdgcn_exp2f(v); }

DEV void dma16(const void* sbase, uint32_t voff, uint32_t lds_dst) {
    unsigned keep;
    asm volatile("s_mov_b32 %0, m0\n\ts_mov_b32 m0, %3\n\ts_nop 0\n\tglobal_load_lds_dwordx4 %1, %2\n\ts_mov_b32 m0, %0"
                 : "=&s"(keep) : "v"(voff), "s"(sbase), "s"(lds_dst) : "memory");
}
DEV void dma16nt(const void* sbase, uint32_t voff, uint32_t lds_dst) {
    unsigned keep;
    asm volatile("s_mov_b32 %0, m0\n\ts_mov_b32 m0, %3\n\ts_nop 0\n\tglobal_load_lds_dwordx4 %1, %2 nt\n\ts_mov_b32 m0, %0"
                 : "=&s"(keep) : "v"(voff), "s"(sbase), "s"(lds_dst) : "memory");
}
DEV uint32_t gload32(const void* sbase, uint32_t voff) {
    uint32_t v;
    asm volatile("global_load_dword %0, %1, %2" : "=v"(v) : "v"(voff), "s"(sbase) : "memory");
    return v;
}
DEV void gstore128(void* sbase, uint32_t voff, u32x4 v) {
    asm volatile("global_store_dwordx4 %0, %1, %2 sc0 sc1" :: "v"(voff), "v"(v), "s"(sbase) : "memory");
}
#define BARRIER() do { asm volatile("s_waitcnt lgkmcnt(0)" ::: "memory"); __builtin_amdgcn_s_barrier(); asm volatile("" ::: "memory"); } while (0)
#define WAIT_VM(n) asm volatile("s_waitcnt vmcnt(" #n ")" ::: "memory")

DEV void stream_sync(lds_ptr cnt, uint32_t target) {
    asm volatile("s_waitcnt lgkmcnt(0)" ::: "memory");
    if ((threadIdx.x & 63) == 0) __hip_atomic_fetch_add((LAS uint32_t*)cnt, 1u, __ATOMIC_RELAXED, __HIP_MEMORY_SCOPE_WORKGROUP);
    while (true) {
        uint32_t v = __hip_atomic_load((LAS uint32_t*)cnt, __ATOMIC_RELAXED, __HIP_MEMORY_SCOPE_WORKGROUP);
        if (__builtin_amdgcn_readfirstlane(v) >= target) break;
        __builtin_amdgcn_s_sleep(1);
    }
    asm volatile("" ::: "memory");
}

struct Frags {
    u32x4 Q[3], K[4];
    u32x4 VT[2][2];
};

DEV void pv_norm(const Frags& F, u32x4 P0, u32x4 P1, uint32_t (&o)[3], uint32_t& sumbits) {
    f32x4 zero = {0.f, 0.f, 0.f, 0.f};
    f32x4 oa = mfma(F.VT[0][0], P0, zero);
    f32x4 ob = mfma(F.VT[1][0], P0, zero);
    oa = mfma(F.VT[0][1], P1, oa);
    ob = mfma(F.VT[1][1], P1, ob);
    float sum = ob[2];
    sumbits = __builtin_bit_cast(uint32_t, sum);
    float rs = __builtin_amdgcn_rcpf(sum);
    o[0] = pk(oa[0] * rs, oa[1] * rs);
    o[1] = pk(oa[2] * rs, oa[3] * rs);
    o[2] = pk(ob[0] * rs, ob[1] * rs);
}

template <bool SAFE>
DEV void attn_tile(const Frags& F, u32x4 Q, const f32x4 (&bias)[3], f32x4 CW, lds_ptr M, int ma0, int ma1, int ma2,
                   u32x4 IHL, uint32_t (&o)[3], uint32_t& sumbits) {
    f32x4 s0, s1, s2;
    float s30;
    if (!SAFE) {
        s0 = mfma(F.K[0], Q, bias[0]);
        s1 = mfma(F.K[1], Q, bias[1]);
        s2 = mfma(F.K[2], Q, bias[2]);
        f32x4 s3 = mfma(F.K[3], Q, CW);
        s30 = s3[0];
        u32x4 m0 = lds_r128(M + ma0), m1 = lds_r128(M + ma1), m2 = lds_r128(M + ma2);
        s0 = mfma(IHL, m0, s0);
        s1 = mfma(IHL, m1, s1);
        s2 = mfma(IHL, m2, s2);
    } else {
        const int lane = threadIdx.x & 63;
        const int g = lane >> 4, mm = lane & 15;
        const int v = (mm >> 1) & 7;
        const u32x4 Qz = Q;
        f32x4 zero = {0.f, 0.f, 0.f, 0.f};
        s0 = mfma(F.K[0], Qz, bias[0]);
        s1 = mfma(F.K[1], Qz, bias[1]);
        s2 = mfma(F.K[2], Qz, bias[2]);
        f32x4 s3 = mfma(F.K[3], Qz, zero);
        const int h4 = 4 * (__builtin_amdgcn_readfirstlane(threadIdx.x >> 6) & 3);
        const uint32_t bb = *(const LAS uint32_t*)(M + 128 * mm + 16 * (7 ^ v) + h4);
        const float m48 = bflo(lds_r16(M + 128 * mm + 16 * (6 ^ v) + 14));
        s30 = __builtin_fmaf(m48, LOG2E, s3[0] + (bflo(bb) + bfhi(bb)));
        u32x2 mv0 = lds_r64(M + 128 * mm + 16 * ((0 + (g >> 1)) ^ v) + 8 * (g & 1));
        u32x2 mv1 = lds_r64(M + 128 * mm + 16 * ((2 + (g >> 1)) ^ v) + 8 * (g & 1));
        u32x2 mv2 = lds_r64(M + 128 * mm + 16 * ((4 + (g >> 1)) ^ v) + 8 * (g & 1));
#define ADDM(s, mv) s[0] = __builtin_fmaf(bflo(mv[0]), LOG2E, s[0]); s[1] = __builtin_fmaf(bfhi(mv[0]), LOG2E, s[1]); \
                    s[2] = __builtin_fmaf(bflo(mv[1]), LOG2E, s[2]); s[3] = __builtin_fmaf(bfhi(mv[1]), LOG2E, s[3]);
        ADDM(s0, mv0) ADDM(s1, mv1) ADDM(s2, mv2)
#undef ADDM
        s30 = (CW[0] == 0.f) ? s30 : -INFINITY;
        float mx = s30;
#pragma unroll
        for (int r = 0; r < 4; ++r) mx = fmaxf(mx, fmaxf(s0[r], fmaxf(s1[r], s2[r])));
        mx = fmaxf(mx, __shfl_xor(mx, 16));
        mx = fmaxf(mx, __shfl_xor(mx, 32));
#pragma unroll
        for (int r = 0; r < 4; ++r) { s0[r] -= mx; s1[r] -= mx; s2[r] -= mx; }
        s30 -= mx;
    }
    u32x4 P0 = {pk(ex2(s0[0]), ex2(s0[1])), pk(ex2(s0[2]), ex2(s0[3])), pk(ex2(s1[0]), ex2(s1[1])), pk(ex2(s1[2]), ex2(s1[3]))};
    u32x4 P1 = {pk(ex2(s2[0]), ex2(s2[1])), pk(ex2(s2[2]), ex2(s2[3])), pk(ex2(s30), 0.f), 0u};
    pv_norm(F, P0, P1, o, sumbits);
}

template <bool SAFE>
DEV void attn_left(const Frags& F, float cinit, bool valid, lds_ptr QLw, const int (&qa)[4], lds_ptr PL, uint32_t (&o)[3],
                   uint32_t& sumbits) {
    const int lane = threadIdx.x & 63;
    const int g = lane >> 4;
    f32x4 s = {cinit, 0.f, 0.f, 0.f};
#pragma unroll
    for (int kt = 0; kt < 4; ++kt) s = mfma(lds_r128(QLw + qa[kt]), F.K[kt], s);
    float v = s[0];
    if (SAFE) {
        v = valid ? v : -INFINITY;
        float mx = v;
#pragma unroll
        for (int d = 1; d < 64; d <<= 1) mx = fmaxf(mx, __shfl_xor(mx, d));
        v -= mx;
    }
    *(LAS uint16_t*)(PL + 2 * lane) = (uint16_t)pk(ex2(v), 0.f);
    u32x2 a0 = lds_r64(PL + 8 * g), a1 = lds_r64(PL + 8 * g + 32), a2 = lds_r64(PL + 8 * g + 64), a3 = lds_r64(PL + 8 * g + 96);
    u32x4 P0 = {a0[0], a0[1], a1[0], a1[1]}, P1 = {a2[0], a2[1], a3[0], a3[1]};
    pv_norm(F, P0, P1, o, sumbits);
}

__global__ __launch_bounds__(512, 2) void wattn_kernel(const uint16_t* __restrict__ x, const uint16_t* __restrict__ wqkv,
                                                       const uint16_t* __restrict__ wo, const uint16_t* __restrict__ rpb,
                                                       const int* __restrict__ rpidx, const uint16_t* __restrict__ mask,
                                                       uint16_t* __restrict__ out) {
    extern __shared__ __attribute__((aligned(16))) char smem_[];
    lds_ptr L = (lds_ptr)smem_;
    const uint32_t Lbase = (uint32_t)(uintptr_t)L;
    const int tid = threadIdx.x, lane = tid & 63;
    const int wv = __builtin_amdgcn_readfirstlane(tid >> 6);
    const int h = wv & 3, p = wv >> 2;
    const int g = lane >> 4, m = lane & 15;
    const int blk = blockIdx.x;
    const int wh = blk >> 3, ww0 = (blk & 7) * 4;

    const uint32_t rsrc = 4 * (tid < 338 ? tid : 337);
    uint32_t isrc[2];
    bool iok[2];
#pragma unroll
    for (int j = 0; j < 2; ++j) {
        const int pos = 80 * wv + 64 * j + lane;
        const int q = pos / 13, c = pos - 13 * q;
        isrc[j] = (uint32_t)(196 * q + (c < 12 ? 16 * c : 180));
        iok[j] = (j == 0 || lane < 16) && pos < 637;
    }
    int xrel[3], xwr[3];
#pragma unroll
    for (int j = 0; j < 3; ++j) {
        const int pw = 64 * j + lane;
        const int pos = 147 * h + (pw < 147 ? pw : 146);
        const int t = pos / 12, c = pos - 12 * t;
        const int a = t / 7, b = t - 7 * a;
        xrel[j] = (a * 224 + b) * 192 + ((16 * c) ^ ((t << 2) & 32)) + ((wh == 31 && a >= 4) ? -9633792 : 0);
        xwr[j] = (b >= 4) ? -43008 : 0;
    }
    uint32_t wsrc[3];
    bool wok[3];
#pragma unroll
    for (int j = 0; j < 3; ++j) {
        const int sl = 64 * j + lane;
        const int r4 = sl / 38, c = sl - 38 * r4;
        wsrc[j] = 576 * r4 + 16 * c;
        wok[j] = c < 36 && sl < 152;
    }
    uint32_t msrc[2];
    bool mok[2];
#pragma unroll
    for (int j = 0; j < 2; ++j) {
        const int pos = 98 * h + 64 * j + lane;
        const int q = pos >> 3, mk = (pos & 7) ^ ((q >> 1) & 7);
        msrc[j] = 98 * q + (mk < 6 ? 16 * mk : 82);
        mok[j] = (mk < 7) && (j == 0 || lane < 34);
    }
    asm volatile("" :: "v"(isrc[0]), "v"(isrc[1]), "v"(xrel[0]), "v"(xrel[1]), "v"(xrel[2]), "v"(xwr[0]), "v"(xwr[1]), "v"(xwr[2]));
    asm volatile("" :: "v"(wsrc[0]), "v"(wsrc[1]), "v"(wsrc[2]), "v"(msrc[0]), "v"(msrc[1]), "v"(rsrc));
    auto load_x = [&](int i) {
        const int ww = ww0 + i;
        const int org = ((7 * wh + 3) * 224 + 7 * ww + 3) * 192;
        const uint32_t dst = Lbase + OFF_X + i * XS + 147 * 16 * h;
        const bool wr = ww == 31;
        dma16nt(x, (uint32_t)(org + xrel[0] + (wr ? xwr[0] : 0)), dst);
        dma16nt(x, (uint32_t)(org + xrel[1] + (wr ? xwr[1] : 0)), dst + 1024);
        if (lane < 19) dma16nt(x, (uint32_t)(org + xrel[2] + (wr ? xwr[2] : 0)), dst + 2048);
    };
    auto load_w = [&](int s) {
        const int kb = 4 * (wv ^ ((wv >> 1) & 1));
        const char* src = (const char*)wqkv + (32 * s + kb) * 576;
#pragma unroll
        for (int j = 0; j < 3; ++j)
            if (wok[j]) dma16(src, wsrc[j], Lbase + OFF_W + (32 * s + 4 * wv) * WP + 1024 * j);
    };
    auto load_mask = [&](int i) {
        const char* mb = (const char*)mask + (size_t)(4 * blk + i) * 4802;
        const uint32_t dst = Lbase + OFF_MASK + i * MS + 98 * 16 * h;
        if (mok[0]) dma16nt(mb, msrc[0], dst);
        if (mok[1]) dma16nt(mb, msrc[1], dst + 1024);
    };
    auto load_wo = [&]() {
        const uint32_t o = 2304 * wv + 16 * lane;
        dma16(wo, o, Lbase + OFF_WORAW + 2304 * wv);
        dma16(wo, o + 1024, Lbase + OFF_WORAW + 2304 * wv + 1024);
        if (lane < 16) dma16(wo, o + 2048, Lbase + OFF_WORAW + 2304 * wv + 2048);
    };
    load_x(p);
    {
        int y = 7 * wh + 9; if (y >= 224) y -= 224;
        int xx = 7 * (ww0 + (wv >> 1)) + 9; if (xx >= 224) xx -= 224;
        if (lane < 6) dma16nt(x, (uint32_t)((y * 224 + xx) * 192 + 96 * (wv & 1) + 16 * lane), Lbase + OFF_XL + 96 * wv);
    }
    uint32_t rpbv = gload32(rpb, rsrc);
    if (iok[0]) dma16(rpidx, isrc[0], Lbase + OFF_IDX + 16 * (80 * wv));
    if (iok[1]) dma16(rpidx, isrc[1], Lbase + OFF_IDX + 16 * (80 * wv + 64));
    load_w(0);
    load_w(1);
    if (tid < 192) lds_w128(L + OFF_W + (tid >> 1) * WP + 576 + 16 * (tid & 1), u32x4{0u, 0u, 0u, 0u});
    if (wv == 6) lds_w32(L + OFF_ZERO + 4 * lane, 0u);
    if (wv == 7 && lane < 2) lds_w32(L + OFF_CNT + 4 * lane, 0u);

    const float onec = ((m & 3) >= 2) ? 1.0f : 0.0f;
    const f32x4 ONES = {onec, onec, onec, onec};
    const f32x4 ZERO = {0.f, 0.f, 0.f, 0.f};
    const float qsc = (g & 1) ? 1.0f : QSCALE;
    const int xsw = (16 * g) ^ (((m >> 3) & 1) << 5);
    lds_ptr QL = L + OFF_QL + wv * 256;
    lds_ptr PL = L + OFF_PL + wv * 128;
    const int role = (h + 2 * p) & 3;
    lds_ptr CNT = L + OFF_CNT + 4 * p;
    int wa1, wa2, wc, wv1;
    {
        const int q4 = m >> 2, p4 = m & 3;
        wa1 = OFF_W + WP * (8 * g + q4 + 4 * (g & 1));
        wa2 = OFF_W + WP * (8 * g + q4 + 4 * (1 - (g & 1)));
        wc = 48 * h + 32 + 8 * (p4 >> 1) + 192 * (p4 & 1);
        wv1 = (p4 < 2) ? 384 + 48 * h + 32 + 8 * p4 : 576 + 8 * (p4 - 2);
        wa1 += 0; wa2 += 0;
    }
    const int wab = 48 * h + 8 * (m & 3);
    const int bperm = 4 * (16 * g + (((m & 3) < 2) ? 2 * (m >> 2) + (m & 1) : 8));
    u32x4 Wf[5][3];
    auto load_wf = [&](int s) {
        const int so = 32 * WP * s;
        auto fr = [&](int off) -> u32x4 {
            u32x2 lo = lds_tr(L + wa1 + off + so), hi = lds_tr(L + wa2 + off + so);
            return u32x4{lo[0], lo[1], hi[0], hi[1]};
        };
        Wf[0][s] = fr(wab);
        Wf[1][s] = fr(wab + 192);
        Wf[2][s] = fr(wc);
        Wf[3][s] = fr(wab + 384);
        const u32x4 t = fr(wv1);
#pragma unroll
        for (int r = 0; r < 4; ++r) Wf[4][s][r] = (uint32_t)__builtin_amdgcn_ds_bpermute(bperm, (int)t[r]);
    };
    struct Acc { f32x4 a0, a1, a2, v0, v1; };
    auto proj_step = [&](Acc& A, u32x4 xf, int s, bool first) {
        A.a0 = mfma(Wf[0][s], xf, first ? ZERO : A.a0);
        A.a1 = mfma(Wf[1][s], xf, first ? ZERO : A.a1);
        A.a2 = mfma(Wf[2][s], xf, first ? ZERO : A.a2);
        A.v0 = mfma(xf, Wf[3][s], first ? ZERO : A.v0);
        A.v1 = mfma(xf, Wf[4][s], first ? ONES : A.v1);
    };
    auto proj_fin = [&](const Acc& A, u32x4& Q, u32x4& K, u32x2& V0, u32x2& V1) {
        const uint32_t c0 = pk(A.a2[0] * qsc, A.a2[1] * qsc), c1 = pk(A.a2[2] * qsc, A.a2[3] * qsc);
        const auto sw = __builtin_amdgcn_permlane16_swap(c0, c1, false, false);
        Q[0] = pk(A.a0[0] * QSCALE, A.a0[1] * QSCALE); Q[1] = pk(A.a0[2] * QSCALE, A.a0[3] * QSCALE); Q[2] = sw[0];
        K[0] = pk(A.a1[0], A.a1[1]); K[1] = pk(A.a1[2], A.a1[3]); K[2] = sw[1];
        V0 = u32x2{pk(A.v0[0], A.v0[1]), pk(A.v0[2], A.v0[3])};
        V1 = u32x2{pk(A.v1[0], A.v1[1]), pk(A.v1[2], A.v1[3])};
    };
    auto project = [&](lds_ptr xa, u32x4& Q, u32x4& K, u32x2& V0, u32x2& V1) {
        u32x4 xf0 = lds_r128(xa), xf1 = lds_r128(xa + 64), xf2 = lds_r128(xa + 128);
        Acc A;
        proj_step(A, xf0, 0, true); proj_step(A, xf1, 1, false); proj_step(A, xf2, 2, false);
        proj_fin(A, Q, K, V0, V1);
    };
    Frags F;
    F.Q[0][3] = F.Q[1][3] = F.Q[2][3] = 0u;
    F.K[0][3] = F.K[1][3] = F.K[2][3] = 0u;
    F.K[3][3] = (g == 0) ? 0x3FB90000u : (g == 1) ? 0xBB2C0000u : (g == 2) ? 0x3F803F80u : 0u;
    u32x2 V0L, V1L;
    f32x4 bias[3][3];
    float biasL;
    u32x2 V0[3], V1[3];

    {
        lds_ptr X = L + OFF_X + p * XS;
        lds_ptr xl = L + OFF_XL + 192 * (m >> 2) + 16 * g;
        lds_ptr xa = X + 192 * m + xsw;
        Acc A[4];
        asm volatile("s_waitcnt vmcnt(8)" : "+v"(rpbv) :: "memory");
        if (tid < 338) {
            lds_ptr d = L + OFF_RPB + (tid & 1) * 1408 + (tid >> 1) * 4;
            *(LAS float*)d = bflo(rpbv) * LOG2E;
            *(LAS float*)(d + 704) = bfhi(rpbv) * LOG2E;
        }
        WAIT_VM(3);
        BARRIER();
        load_w(2);
        load_mask(p);
        load_wf(0);
        proj_step(A[3], lds_r128(xl), 0, true);
#pragma unroll
        for (int tt = 0; tt < 3; ++tt) proj_step(A[tt], lds_r128(xa + 3072 * tt), 0, true);
        {
            lds_ptr T = L + OFF_RPB + 704 * h;
#pragma unroll
            for (int qt = 0; qt < 3; ++qt)
#pragma unroll
                for (int kt = 0; kt < 3; ++kt) {
                    const u32x4 iv = lds_r128(L + OFF_IDX + 208 * (16 * qt + m) + 64 * kt + 16 * g);
#pragma unroll
                    for (int r = 0; r < 4; ++r) bias[qt][kt][r] = *(const LAS float*)(T + 4 * iv[r]);
                }
            const int l48 = lane < 48 ? lane : 48;
            const float b48 = *(const LAS float*)(T + 4 * *(const LAS uint32_t*)(L + OFF_IDX + 208 * l48 + 204));
            biasL = *(const LAS float*)(T + 4 * *(const LAS uint32_t*)(L + OFF_IDX + 208 * 48 + (lane < 48 ? 4 * lane : 204)));
            const uint32_t hi = pk(b48, 0.f) & 0xffffu;
            const uint32_t pr = hi | (pk(b48 - bflo(hi), 0.f) << 16);
            const int a48 = 128 * lane + 16 * (7 ^ ((lane >> 1) & 7)) + 4 * h;
            if (lane < 48) {
                lds_w32(L + OFF_MASK + p * MS + a48, pr);
                lds_w32(L + OFF_MASK + (p + 2) * MS + a48, pr);
            }
        }
        WAIT_VM(5);
        BARRIER();
        load_wo();
        load_x(p + 2);
        load_wf(1);
        proj_step(A[3], lds_r128(xl + 64), 1, false);
#pragma unroll
        for (int tt = 0; tt < 3; ++tt) proj_step(A[tt], lds_r128(xa + 3072 * tt + 64), 1, false);
        WAIT_VM(8);
        BARRIER();
        load_mask(p + 2);
        load_wf(2);
        proj_step(A[3], lds_r128(xl + 128), 2, false);
#pragma unroll
        for (int tt = 0; tt < 3; ++tt) proj_step(A[tt], lds_r128(xa + 3072 * tt + 128), 2, false);
        u32x4 QLr = {0u, 0u, 0u, 0u};
        proj_fin(A[3], QLr, F.K[3], V0L, V1L);
        if ((m & 3) == 0) lds_w128(QL + (m >> 2) * 64 + g * 16, QLr);
#pragma unroll
        for (int tt = 0; tt < 3; ++tt) proj_fin(A[tt], F.Q[tt], F.K[tt], V0[tt], V1[tt]);
        WAIT_VM(5);
        BARRIER();
    }

    int ln = lane;
    asm volatile("" : "+v"(ln));
    const int g2 = ln >> 4, m2 = ln & 15;
    u32x4 IHL;
    {
        const uint32_t val = (g2 < 2) ? 0x3FB9u : 0xBB2Cu;
        const int j = m2 - 8 * (g2 & 1);
#pragma unroll
        for (int r = 0; r < 4; ++r) IHL[r] = (j == 2 * r) ? val : (j == 2 * r + 1) ? (val << 16) : 0u;
    }
    const int vsw = (m2 >> 1) & 7;
    const int ma0 = 128 * m2 + 16 * ((0 + (g2 & 1)) ^ vsw), ma1 = 128 * m2 + 16 * ((2 + (g2 & 1)) ^ vsw), ma2 = 128 * m2 + 16 * ((4 + (g2 & 1)) ^ vsw);
    const int q3a = 128 * m2 + (g2 < 2 ? 16 * (6 ^ vsw) + 12 : 16 * (7 ^ vsw) + 4 * h);
    const int mLa = 128 * 48 + (ln < 48 ? 2 * ln : 110);
    int qa[4];
#pragma unroll
    for (int kt = 0; kt < 4; ++kt) qa[kt] = ((m2 >> 2) == kt) ? 16 * g : (OFF_ZERO - OFF_QL - wv * 256 + 16 * g2);
    int woa0, woa2;
    {
        const int q4 = m2 >> 2, p4 = m2 & 3;
        const int rb = 24 * ((q4 >> 1) ^ (g2 & 1)) + (q4 & 1);
        woa0 = OFF_WORAW + (rb + 4 * g2) * 192 + 16 * p4;
        woa2 = OFF_WORAW + (rb + 16 + 2 * g2) * 192 + 16 * p4;
    }
    auto wo_frag = [&](int colb, int s) -> u32x4 {
        lds_ptr a = L + (s == 2 ? woa2 : woa0 + (s == 1 ? 384 : 0)) + colb;
        u32x2 lo = lds_tr(a), hi = lds_tr(a + 48 * 192);
        return u32x4{lo[0], lo[1], hi[0], hi[1]};
    };

    auto iter = [&](auto it_tag) {
        constexpr int it = decltype(it_tag)::value;
        const int i = p + 2 * it;
        const int ww = ww0 + i;
        lds_ptr X = L + OFF_X + i * XS;
        lds_ptr M = L + OFF_MASK + i * MS;
        lds_ptr OB = L + OFF_OB + i * OBS;
        lds_ptr OL = L + OFF_OL + p * 3072;
        uint32_t o[4][3];
        const f32x4 CW = {(g == i) ? 0.f : NEG_BIG, 0.f, 0.f, 0.f};
        const bool validL = (lane < 48) || (lane == 48 + 4 * i);
        auto window = [&](auto safe_tag, auto skip_tag) -> bool {
            constexpr bool SAFE = decltype(safe_tag)::value;
            constexpr bool SKIP_PROJ = decltype(skip_tag)::value;
            if (!SKIP_PROJ) {
#pragma unroll
                for (int tt = 0; tt < 3; ++tt) project(X + 192 * (16 * tt + m) + xsw, F.Q[tt], F.K[tt], V0[tt], V1[tt]);
            }
            F.VT[0][0] = u32x4{V0[0][0], V0[0][1], V0[1][0], V0[1][1]};
            F.VT[0][1] = u32x4{V0[2][0], V0[2][1], V0L[0], V0L[1]};
            F.VT[1][0] = u32x4{V1[0][0], V1[0][1], V1[1][0], V1[1][1]};
            F.VT[1][1] = u32x4{V1[2][0], V1[2][1], V1L[0], V1L[1]};
#pragma unroll
            for (int tt = 0; tt < 3; ++tt) F.Q[tt][3] = SAFE ? 0u : *(const LAS uint32_t*)(M + 2048 * tt + q3a);
            uint32_t sb[4];
            const float mL = bflo(lds_r16(M + mLa));
            const float cL = validL ? __builtin_fmaf(mL, LOG2E, biasL) : NEG_BIG;
#pragma unroll
            for (int qt = 0; qt < 3; ++qt)
                attn_tile<SAFE>(F, F.Q[qt], bias[qt], CW, M + 2048 * qt, ma0, ma1, ma2, IHL, o[qt], sb[qt]);
            attn_left<SAFE>(F, cL, validL, QL + 64 * i, qa, PL, o[3], sb[3]);
            const uint32_t lo = min(min(sb[0], sb[1]), min(sb[2], sb[3])), hi = max(max(sb[0], sb[1]), max(sb[2], sb[3]));
            const bool bad = (lo < 0x2B800000u) || (hi > 0x53800000u);
            return __builtin_amdgcn_ballot_w64(bad) != 0ull;
        };
        if (window(std::false_type{}, std::integral_constant<bool, it == 0>{})) (void)window(std::true_type{}, std::false_type{});
        {
            const int osw = lane * 16 + 4 * (h ^ (g & 1));
#pragma unroll
            for (int qt = 0; qt < 3; ++qt)
#pragma unroll
                for (int s = 0; s < 3; ++s) lds_w32(OB + osw + qt * 3072 + s * 1024, o[qt][s]);
            if (m == it) {
#pragma unroll
                for (int s = 0; s < 3; ++s) lds_w32(OL + osw + s * 1024, o[3][s]);
            }
        }
        if (it == 0) WAIT_VM(0);
        stream_sync(CNT, 4 * (it + 1));
        int y0 = 7 * wh + 3;
        const int x0 = 7 * ww + 3;
        if (role < 3) {
            const int cb = 64 * role;
            u32x4 w0 = wo_frag(cb, 0), w1 = wo_frag(cb, 1), w2 = wo_frag(cb, 2);
            u32x4 w3 = wo_frag(cb + 8, 0), w4 = wo_frag(cb + 8, 1), w5 = wo_frag(cb + 8, 2);
#pragma unroll
            for (int qt = 0; qt < 3; ++qt) {
                lds_ptr orr = OB + qt * 3072 + lane * 16;
                u32x4 of0 = lds_r128(orr), of1 = lds_r128(orr + 1024), of2 = lds_r128(orr + 2048);
                const int tok = 16 * qt + m;
                const int a = (tok * 37) >> 8, b = tok - 7 * a;
                int y = y0 + a; if (y >= 224) y -= 224;
                int xx = x0 + b; if (xx >= 224) xx -= 224;
                f32x4 c0 = mfma(w0, of0, ZERO), c1 = mfma(w3, of0, ZERO);
                c0 = mfma(w1, of1, c0); c1 = mfma(w4, of1, c1);
                c0 = mfma(w2, of2, c0); c1 = mfma(w5, of2, c1);
                u32x4 r = {pk(c0[0], c0[1]), pk(c0[2], c0[3]), pk(c1[0], c1[1]), pk(c1[2], c1[3])};
                gstore128(out, (uint32_t)((y * 224 + xx) * 192 + 16 * g + 64 * role), r);
            }
        } else if (it == 1) {
            lds_ptr orr = OL + lane * 16;
            u32x4 of0 = lds_r128(orr), of1 = lds_r128(orr + 1024), of2 = lds_r128(orr + 2048);
            int y = y0 + 6; if (y >= 224) y -= 224;
            int xx = 7 * (ww0 + p + 2 * (m & 1)) + 9; if (xx >= 224) xx -= 224;
            char* dst = (char*)out + (y * 224 + xx) * 192 + 16 * g;
#pragma unroll
            for (int i2 = 0; i2 < 3; ++i2) {
                f32x4 c0 = mfma(wo_frag(64 * i2, 0), of0, ZERO);
                f32x4 c1 = mfma(wo_frag(64 * i2 + 8, 0), of0, ZERO);
                c0 = mfma(wo_frag(64 * i2, 1), of1, c0);
                c1 = mfma(wo_frag(64 * i2 + 8, 1), of1, c1);
                c0 = mfma(wo_frag(64 * i2, 2), of2, c0);
                c1 = mfma(wo_frag(64 * i2 + 8, 2), of2, c1);
                u32x4 r = {pk(c0[0], c0[1]), pk(c0[2], c0[3]), pk(c1[0], c1[1]), pk(c1[2], c1[3])};
                if (m < 2) gstore128(out, (uint32_t)((y * 224 + xx) * 192 + 16 * g + 64 * i2), r);
            }
        }
    };
    iter(std::integral_constant<int, 0>{});
    iter(std::integral_constant<int, 1>{});
}

extern "C" void kernel_launch(void* const* d_in, const int* in_sizes, int n_in, void* d_out, int out_size, void* d_ws,
                              size_t ws_size, hipStream_t stream) {
    (void)in_sizes; (void)n_in; (void)out_size; (void)d_ws; (void)ws_size;
    (void)hipFuncSetAttribute((const void*)wattn_kernel, hipFuncAttributeMaxDynamicSharedMemorySize, LDS_TOTAL);
    wattn_kernel<<<256, 512, LDS_TOTAL, stream>>>((const uint16_t*)d_in[0], (const uint16_t*)d_in[1], (const uint16_t*)d_in[2],
                                                  (const uint16_t*)d_in[3], (const int*)d_in[4], (const uint16_t*)d_in[5],
                                                  (uint16_t*)d_out);
}
```

```cpp
#include <hip/hip_runtime.h>
#include <hip/hip_bf16.h>
#include <stdint.h>
#include <type_traits>

typedef __attribute__((ext_vector_type(8))) __bf16 bf16x8;
typedef __attribute__((ext_vector_type(2))) __bf16 bf16x2;
typedef __attribute__((ext_vector_type(4))) float f32x4;
typedef __attribute__((ext_vector_type(2))) float f32x2;
typedef __attribute__((ext_vector_type(4))) uint32_t u32x4;
typedef __attribute__((ext_vector_type(2))) uint32_t u32x2;
typedef __attribute__((ext_vector_type(4))) short s16x4;

#define DEV __device__ __forceinline__
#define LAS __attribute__((address_space(3)))
typedef LAS char* lds_ptr;

constexpr int OFF_W = 0;
constexpr int OFF_OB = 0, OBS = 9216;
constexpr int OFF_OL = 36864;
constexpr int OFF_RPB = 55296;
constexpr int OFF_X = 58016, XS = 9216;
constexpr int OFF_XL = OFF_X + 4 * XS;
constexpr int OFF_MASK = OFF_XL + 768, MS = 6272;
constexpr int OFF_WO = OFF_MASK + 4 * MS;
constexpr int OFF_BH = OFF_WO;
constexpr int OFF_QL = OFF_BH + 36864;
constexpr int OFF_ZERO = OFF_QL + 2048;
constexpr int OFF_PL = OFF_ZERO + 256;
constexpr int OFF_BL = OFF_PL + 1024;
constexpr int OFF_CNT = OFF_BL + 1024;
constexpr int LDS_TOTAL = OFF_CNT + 64;
static_assert(LDS_TOTAL <= 163840 - 1024, "LDS");
static_assert(OFF_X % 16 == 0 && OFF_MASK % 16 == 0 && OFF_WO % 16 == 0 && OFF_QL % 16 == 0, "align");

constexpr float LOG2E = 1.4426950408889634f;
constexpr float QSCALE = 0.20412414523193154f * 1.4426950408889634f;
constexpr float NEG_BIG = -1.0e30f;

DEV uint32_t pk(float a, float b) {
    f32x2 v = {a, b};
    bf16x2 r = __builtin_convertvector(v, bf16x2);
    return __builtin_bit_cast(uint32_t, r);
}
DEV uint32_t pks(float a, float b, float s) { return pk(a * s, __builtin_fmaf(b, s, 0.0f)); }
DEV f32x4 mfma(u32x4 a, u32x4 b, f32x4 c) {
    return __builtin_amdgcn_mfma_f32_16x16x32_bf16(__builtin_bit_cast(bf16x8, a), __builtin_bit_cast(bf16x8, b), c, 0, 0, 0);
}
DEV u32x4 lds_r128(lds_ptr p) { return *(const LAS u32x4*)p; }
DEV f32x4 lds_rf128(lds_ptr p) { return *(const LAS f32x4*)p; }
DEV u32x2 lds_r64(lds_ptr p) { return *(const LAS u32x2*)p; }
DEV uint32_t lds_r32(lds_ptr p) { return *(const LAS uint32_t*)p; }
DEV float lds_rf(lds_ptr p) { return *(const LAS float*)p; }
DEV uint32_t lds_r16(lds_ptr p) { return (uint32_t) * (const LAS uint16_t*)p; }
DEV void lds_w128(lds_ptr p, u32x4 v) { *(LAS u32x4*)p = v; }
DEV void lds_wf128(lds_ptr p, f32x4 v) { *(LAS f32x4*)p = v; }
DEV void lds_w32(lds_ptr p, uint32_t v) { *(LAS uint32_t*)p = v; }
DEV void lds_w16(lds_ptr p, uint32_t v) { *(LAS uint16_t*)p = (uint16_t)v; }
DEV u32x2 lds_tr(lds_ptr p) {
    return __builtin_bit_cast(u32x2, __builtin_amdgcn_ds_read_tr16_b64_v4i16((LAS s16x4*)p));
}
DEV float bflo(uint32_t v) { return __builtin_bit_cast(float, v << 16); }
DEV float bfhi(uint32_t v) { return __builtin_bit_cast(float, v & 0xffff0000u); }
DEV float ex2(float v) { return __builtin_amdgcn_exp2f(v); }

DEV void dma16nt(const void* sbase, uint32_t voff, uint32_t lds_dst) {
    unsigned keep;
    asm volatile("s_mov_b32 %0, m0\n\ts_mov_b32 m0, %3\n\ts_nop 0\n\tglobal_load_lds_dwordx4 %1, %2 nt\n\ts_mov_b32 m0, %0"
                 : "=&s"(keep) : "v"(voff), "s"(sbase), "s"(lds_dst) : "memory");
}
DEV void dma144(const void* sbase, uint32_t voff, uint32_t lds_dst) {
    unsigned keep;
    asm volatile("s_mov_b32 %0, m0\n\ts_mov_b32 m0, %3\n\ts_nop 0\n\t"
                 "global_load_lds_dwordx4 %1, %2\n\t"
                 "global_load_lds_dwordx4 %1, %2 offset:1024\n\t"
                 "s_mov_b64 exec, 0xffff\n\t"
                 "global_load_lds_dwordx4 %1, %2 offset:2048\n\t"
                 "s_mov_b64 exec, -1\n\ts_mov_b32 m0, %0"
                 : "=&s"(keep) : "v"(voff), "s"(sbase), "s"(lds_dst) : "memory");
}
DEV void dma144nt(const void* sbase, uint32_t v0, uint32_t v1, uint32_t v2, uint32_t lds_dst) {
    unsigned keep;
    asm volatile("s_mov_b32 %0, m0\n\ts_mov_b32 m0, %5\n\ts_nop 0\n\t"
                 "global_load_lds_dwordx4 %1, %4 nt\n\t"
                 "global_load_lds_dwordx4 %2, %4 offset:1024 nt\n\t"
                 "s_mov_b64 exec, 0xffff\n\t"
                 "global_load_lds_dwordx4 %3, %4 offset:2048 nt\n\t"
                 "s_mov_b64 exec, -1\n\ts_mov_b32 m0, %0"
                 : "=&s"(keep) : "v"(v0), "v"(v1), "v"(v2), "s"(sbase), "s"(lds_dst) : "memory");
}
DEV u32x4 gload128(const void* sbase, uint32_t voff) {
    u32x4 v;
    asm volatile("global_load_dwordx4 %0, %1, %2" : "=v"(v) : "v"(voff), "s"(sbase) : "memory");
    return v;
}
DEV uint32_t gload32(const void* sbase, uint32_t voff) {
    uint32_t v;
    asm volatile("global_load_dword %0, %1, %2" : "=v"(v) : "v"(voff), "s"(sbase) : "memory");
    return v;
}
DEV void gstore128(void* sbase, uint32_t voff, u32x4 v) {
    asm volatile("global_store_dwordx4 %0, %1, %2 sc0 sc1" :: "v"(voff), "v"(v), "s"(sbase) : "memory");
}
#define BARRIER() do { asm volatile("s_waitcnt lgkmcnt(0)" ::: "memory"); __builtin_amdgcn_s_barrier(); asm volatile("" ::: "memory"); } while (0)
#define WAIT_VM(n) asm volatile("s_waitcnt vmcnt(" #n ")" ::: "memory")

DEV void arrive(lds_ptr cnt) {
    uint32_t t0, t1;
    asm volatile("s_waitcnt lgkmcnt(0)\n\tv_mov_b32 %0, %2\n\tv_mov_b32 %1, 1\n\ts_mov_b64 exec, 1\n\t"
                 "ds_add_u32 %0, %1\n\ts_mov_b64 exec, -1"
                 : "=&v"(t0), "=&v"(t1) : "s"((uint32_t)(uintptr_t)cnt) : "memory");
}
DEV void wait_cnt(lds_ptr cnt, uint32_t target) {
    asm volatile("" ::: "memory");
    while (true) {
        uint32_t v = __hip_atomic_load((LAS uint32_t*)cnt, __ATOMIC_RELAXED, __HIP_MEMORY_SCOPE_WORKGROUP);
        if (__builtin_amdgcn_readfirstlane(v) >= target) break;
        __builtin_amdgcn_s_sleep(1);
    }
    asm volatile("" ::: "memory");
}

struct Frags {
    u32x4 Q[3], K[4];
    u32x4 VT[2][2];
};

DEV void pv_norm(const Frags& F, u32x4 P0, u32x4 P1, uint32_t (&o)[3], uint32_t& sumbits) {
    f32x4 zero = {0.f, 0.f, 0.f, 0.f};
    f32x4 oa = mfma(F.VT[0][0], P0, zero);
    f32x4 ob = mfma(F.VT[1][0], P0, zero);
    oa = mfma(F.VT[0][1], P1, oa);
    ob = mfma(F.VT[1][1], P1, ob);
    float sum = ob[2];
    sumbits = __builtin_bit_cast(uint32_t, sum);
    float rs = __builtin_amdgcn_rcpf(sum);
    o[0] = pks(oa[0], oa[1], rs);
    o[1] = pks(oa[2], oa[3], rs);
    o[2] = pks(ob[0], ob[1], rs);
}

struct Sc { f32x4 s0, s1, s2; float s30; };
DEV void scores(const Frags& F, u32x4 Q, const f32x4 (&bias)[3], f32x4 CW, lds_ptr M, int ma0, int ma1, int ma2, u32x4 IHL, Sc& S) {
    u32x4 m0 = lds_r128(M + ma0), m1 = lds_r128(M + ma1), m2 = lds_r128(M + ma2);
    S.s0 = mfma(F.K[0], Q, bias[0]);
    S.s1 = mfma(F.K[1], Q, bias[1]);
    S.s2 = mfma(F.K[2], Q, bias[2]);
    f32x4 s3 = mfma(F.K[3], Q, CW);
    S.s30 = s3[0];
    S.s0 = mfma(IHL, m0, S.s0);
    S.s1 = mfma(IHL, m1, S.s1);
    S.s2 = mfma(IHL, m2, S.s2);
}
DEV void exp_pv(const Frags& F, const Sc& S, uint32_t (&o)[3], uint32_t& sumbits) {
    u32x4 P0 = {pk(ex2(S.s0[0]), ex2(S.s0[1])), pk(ex2(S.s0[2]), ex2(S.s0[3])), pk(ex2(S.s1[0]), ex2(S.s1[1])), pk(ex2(S.s1[2]), ex2(S.s1[3]))};
    u32x4 P1 = {pk(ex2(S.s2[0]), ex2(S.s2[1])), pk(ex2(S.s2[2]), ex2(S.s2[3])), pk(ex2(S.s30), 0.f), 0u};
    pv_norm(F, P0, P1, o, sumbits);
}
DEV void scores_safe(const Frags& F, u32x4 Q, const f32x4 (&bias)[3], f32x4 CW, lds_ptr M, Sc& S) {
    const int lane = threadIdx.x & 63;
    const int g = lane >> 4, mm = lane & 15;
    const int v = (mm >> 1) & 7;
    f32x4 zero = {0.f, 0.f, 0.f, 0.f};
    f32x4 s0 = mfma(F.K[0], Q, bias[0]);
    f32x4 s1 = mfma(F.K[1], Q, bias[1]);
    f32x4 s2 = mfma(F.K[2], Q, bias[2]);
    f32x4 s3 = mfma(F.K[3], Q, zero);
    const int h4 = 4 * (__builtin_amdgcn_readfirstlane(threadIdx.x >> 6) & 3);
    const uint32_t bb = lds_r32(M + 128 * mm + 16 * (7 ^ v) + h4);
    const float m48 = bflo(lds_r16(M + 128 * mm + 16 * (6 ^ v) + 14));
    float s30 = __builtin_fmaf(m48, LOG2E, s3[0] + (bflo(bb) + bfhi(bb)));
    u32x2 mv0 = lds_r64(M + 128 * mm + 16 * ((0 + (g >> 1)) ^ v) + 8 * (g & 1));
    u32x2 mv1 = lds_r64(M + 128 * mm + 16 * ((2 + (g >> 1)) ^ v) + 8 * (g & 1));
    u32x2 mv2 = lds_r64(M + 128 * mm + 16 * ((4 + (g >> 1)) ^ v) + 8 * (g & 1));
#define ADDM(s, mv) s[0] = __builtin_fmaf(bflo(mv[0]), LOG2E, s[0]); s[1] = __builtin_fmaf(bfhi(mv[0]), LOG2E, s[1]); \
                    s[2] = __builtin_fmaf(bflo(mv[1]), LOG2E, s[2]); s[3] = __builtin_fmaf(bfhi(mv[1]), LOG2E, s[3]);
    ADDM(s0, mv0) ADDM(s1, mv1) ADDM(s2, mv2)
#undef ADDM
    s30 = (CW[0] == 0.f) ? s30 : -INFINITY;
    float mx = s30;
#pragma unroll
    for (int r = 0; r < 4; ++r) mx = fmaxf(mx, fmaxf(s0[r], fmaxf(s1[r], s2[r])));
    mx = fmaxf(mx, __shfl_xor(mx, 16));
    mx = fmaxf(mx, __shfl_xor(mx, 32));
#pragma unroll
    for (int r = 0; r < 4; ++r) { s0[r] -= mx; s1[r] -= mx; s2[r] -= mx; }
    S.s0 = s0; S.s1 = s1; S.s2 = s2; S.s30 = s30 - mx;
}

template <bool SAFE>
DEV void left_scores(const Frags& F, float cinit, bool valid, lds_ptr QLw, const int (&qa)[4], lds_ptr PL) {
    const int lane = threadIdx.x & 63;
    f32x4 s = {cinit, 0.f, 0.f, 0.f};
#pragma unroll
    for (int kt = 0; kt < 4; ++kt) s = mfma(lds_r128(QLw + qa[kt]), F.K[kt], s);
    float v = s[0];
    if (SAFE) {
        v = valid ? v : -INFINITY;
        float mx = v;
#pragma unroll
        for (int d = 1; d < 64; d <<= 1) mx = fmaxf(mx, __shfl_xor(mx, d));
        v -= mx;
    }
    lds_w16(PL + 2 * lane, pk(ex2(v), 0.f));
}
DEV void left_pv(const Frags& F, lds_ptr PL, uint32_t (&o)[3], uint32_t& sumbits) {
    const int g = (threadIdx.x & 63) >> 4;
    u32x2 a0 = lds_r64(PL + 8 * g), a1 = lds_r64(PL + 8 * g + 32), a2 = lds_r64(PL + 8 * g + 64), a3 = lds_r64(PL + 8 * g + 96);
    u32x4 P0 = {a0[0], a0[1], a1[0], a1[1]}, P1 = {a2[0], a2[1], a3[0], a3[1]};
    pv_norm(F, P0, P1, o, sumbits);
}

__global__ __launch_bounds__(512, 2) void wattn_kernel(const uint16_t* __restrict__ x, const uint16_t* __restrict__ wqkv,
                                                       const uint16_t* __restrict__ wo, const uint16_t* __restrict__ rpb,
                                                       const int* __restrict__ rpidx, const uint16_t* __restrict__ mask,
                                                       uint16_t* __restrict__ out) {
    extern __shared__ __attribute__((aligned(16))) char smem_[];
    lds_ptr L = (lds_ptr)smem_;
    const uint32_t Lbase = (uint32_t)(uintptr_t)L;
    const int tid = threadIdx.x, lane = tid & 63;
    const int wv = __builtin_amdgcn_readfirstlane(tid >> 6);
    const int h = wv & 3, p = wv >> 2;
    const int g = lane >> 4, m = lane & 15;
    const int blk = blockIdx.x;
    const int wh = blk >> 3, ww0 = (blk & 7) * 4;
    const uint32_t wrapY = (wh == 31) ? (uint32_t)-9633792 : 0u;
    const uint32_t org0 = (uint32_t)(((7 * wh + 3) * 224 + 7 * ww0 + 3) * 192);
    const int role = (h + 2 * p) & 3;

    const uint32_t rsrc = 4 * (tid < 338 ? tid : 337);
    const uint32_t l48 = lane < 48 ? lane : 48;
    const uint32_t ilo = 196u * m + 16u * g;
    const int fq = (wv * 11) >> 5;
    const uint32_t isrcA = ilo + (uint32_t)(3136 * fq + 64 * (wv - 3 * fq));
    const uint32_t isrcB = (wv == 4) ? ilo + 6400u : (wv == 5) ? 196u * l48 + 192u : 9408u + 4u * l48;
    uint32_t xr[3];
#pragma unroll
    for (int j = 0; j < 3; ++j) {
        const uint32_t pos = 144u * h + 64u * j + lane;
        const uint32_t a = (pos * 781u) >> 16, p96 = (pos * 683u) >> 16;
        xr[j] = ((pos << 4) ^ ((p96 & 1u) << 5)) + 41664u * a + (pos >= 336u ? wrapY : 0u) + (org0 + 1344u * p + 8192u - 1024u * j);
    }
    const uint32_t wvoff = (16u * lane) ^ (uint32_t)(32 * ((wv >> 1) & 1));
    uint32_t msrc[2];
    bool mok[2];
#pragma unroll
    for (int j = 0; j < 2; ++j) {
        const int pos = 98 * h + 64 * j + lane;
        const int q = pos >> 3, mk = (pos & 7) ^ ((q >> 1) & 7);
        msrc[j] = 98 * q + (mk < 6 ? 16 * mk : 82);
        mok[j] = (mk < 7) && (j == 0 || lane < 34);
    }
    uint32_t so[3];
#pragma unroll
    for (int qt = 0; qt < 3; ++qt) {
        const uint32_t t = 16u * qt + m, a = (t * 37u) >> 8;
        so[qt] = 192u * t + 41664u * a + (a >= 4u ? wrapY : 0u) + 16u * g + (uint32_t)(64 * role);
    }
    if (wv == 6) lds_w32(L + OFF_ZERO + 4 * lane, 0u);
    if (wv == 7 && lane < 2) lds_w32(L + OFF_CNT + 4 * lane, 0u);
    asm volatile("" :: "v"(rsrc), "v"(isrcA), "v"(isrcB), "v"(xr[0]), "v"(xr[1]), "v"(xr[2]), "v"(wvoff), "v"(msrc[0]), "v"(msrc[1]));
    asm volatile("" : "+v"(so[0]), "+v"(so[1]), "+v"(so[2]));
    const char* xm = (const char*)x - 8192;
    auto load_w = [&](int s) {
        dma144((const char*)wqkv + 18432 * s + 2304 * wv, wvoff, Lbase + OFF_W + 18432 * s + 2304 * wv);
    };
    auto load_mask = [&](int i) {
        const char* mb = (const char*)mask + (size_t)(4 * blk + i) * 4802;
        const uint32_t dst = Lbase + OFF_MASK + i * MS + 98 * 16 * h;
        if (mok[0]) dma16nt(mb, msrc[0], dst);
        if (mok[1]) dma16nt(mb, msrc[1], dst + 1024);
    };
    u32x4 ivA, ivB = {0u, 0u, 0u, 0u};
    if (wv == 4) ivB = gload128(rpidx, isrcB);
    else if (wv == 5 || wv == 6) ivB[0] = gload32(rpidx, isrcB);
    uint32_t rpbv = gload32(rpb, rsrc);
    ivA = gload128(rpidx, isrcA);
    {
        int y = 7 * wh + 9; if (y >= 224) y -= 224;
        int xx = 7 * (ww0 + (wv >> 1)) + 9; if (xx >= 224) xx -= 224;
        if (lane < 6) dma16nt(x, (uint32_t)((y * 224 + xx) * 192 + 96 * (wv & 1) + 16 * lane), Lbase + OFF_XL + 96 * wv);
    }
    dma144nt(xm, xr[0], xr[1], xr[2], Lbase + OFF_X + p * XS + 2304 * h);
    load_w(0);
    load_w(1);
    asm volatile("s_waitcnt vmcnt(11)" : "+v"(rpbv), "+v"(ivB) :: "memory");
    if (tid < 338) *(LAS f32x2*)(L + OFF_RPB + 8 * tid) = f32x2{bflo(rpbv) * LOG2E, bfhi(rpbv) * LOG2E};
    load_w(2);
    load_mask(p);

    const float onec = ((m & 3) >= 2) ? 1.0f : 0.0f;
    const f32x4 ONES = {onec, onec, onec, onec};
    const f32x4 ZERO = {0.f, 0.f, 0.f, 0.f};
    const float qsc = (g & 1) ? 1.0f : QSCALE;
    const int xsw = (16 * g) ^ (((m >> 3) & 1) << 5);
    lds_ptr QL = L + OFF_QL + wv * 256;
    lds_ptr PL = L + OFF_PL + wv * 128;
    lds_ptr CNT = L + OFF_CNT + 4 * p;
    lds_ptr wab, wc, wv1;
    {
        const int q4 = m >> 2, p4 = m & 3, sx = 32 * (g & 1);
        lds_ptr rb = L + OFF_W + 576 * (8 * g + q4);
        wab = rb + ((48 * h + 8 * p4) ^ sx);
        wc = rb + ((48 * h + 32 + 8 * (p4 >> 1)) ^ sx) + 192 * (p4 & 1);
        wv1 = rb + 384 + ((48 * h + 32 + 8 * (p4 & 1)) ^ sx);
    }
    const int bperm = 4 * (16 * g + 2 * (m >> 2) + (m & 1));
    const uint32_t bmask = ((m & 3) < 2) ? 0xffffffffu : 0u;
    u32x4 Wf[5][3];
    auto load_wf = [&](int s) {
        auto fr = [&](lds_ptr a) -> u32x4 {
            u32x2 lo = lds_tr(a + 18432 * s), hi = lds_tr(a + 18432 * s + 2304);
            return u32x4{lo[0], lo[1], hi[0], hi[1]};
        };
        Wf[0][s] = fr(wab);
        Wf[1][s] = fr(wab + 192);
        Wf[2][s] = fr(wc);
        Wf[3][s] = fr(wab + 384);
        const u32x4 t = fr(wv1);
#pragma unroll
        for (int r = 0; r < 4; ++r) Wf[4][s][r] = (uint32_t)__builtin_amdgcn_ds_bpermute(bperm, (int)t[r]) & bmask;
    };
    struct Acc { f32x4 a0, a1, a2, v0, v1; };
    auto proj_step = [&](Acc& A, u32x4 xf, int s, bool first) {
        A.a0 = mfma(Wf[0][s], xf, first ? ZERO : A.a0);
        A.a1 = mfma(Wf[1][s], xf, first ? ZERO : A.a1);
        A.a2 = mfma(Wf[2][s], xf, first ? ZERO : A.a2);
        A.v0 = mfma(xf, Wf[3][s], first ? ZERO : A.v0);
        A.v1 = mfma(xf, Wf[4][s], first ? ONES : A.v1);
    };
    auto proj_fin = [&](const Acc& A, u32x4& Q, u32x4& K, u32x2& V0, u32x2& V1) {
        const uint32_t c0 = pks(A.a2[0], A.a2[1], qsc), c1 = pks(A.a2[2], A.a2[3], qsc);
        const auto sw = __builtin_amdgcn_permlane16_swap(c0, c1, false, false);
        Q[0] = pks(A.a0[0], A.a0[1], QSCALE); Q[1] = pks(A.a0[2], A.a0[3], QSCALE); Q[2] = sw[0];
        K[0] = pk(A.a1[0], A.a1[1]); K[1] = pk(A.a1[2], A.a1[3]); K[2] = sw[1];
        V0 = u32x2{pk(A.v0[0], A.v0[1]), pk(A.v0[2], A.v0[3])};
        V1 = u32x2{pk(A.v1[0], A.v1[1]), pk(A.v1[2], A.v1[3])};
    };
    Frags F;
    F.Q[0][3] = F.Q[1][3] = F.Q[2][3] = 0u;
    F.K[0][3] = F.K[1][3] = F.K[2][3] = 0u;
    F.K[3][3] = (g == 0) ? 0x3FB90000u : (g == 1) ? 0xBB2C0000u : (g == 2) ? 0x3F803F80u : 0u;
    u32x2 V0L, V1L;
    f32x4 bias[3][3];
    float biasL;
    u32x2 V0[3], V1[3];
    auto set_vt = [&]() {
        F.VT[0][0] = u32x4{V0[0][0], V0[0][1], V0[1][0], V0[1][1]};
        F.VT[0][1] = u32x4{V0[2][0], V0[2][1], V0L[0], V0L[1]};
        F.VT[1][0] = u32x4{V1[0][0], V1[0][1], V1[1][0], V1[1][1]};
        F.VT[1][1] = u32x4{V1[2][0], V1[2][1], V1L[0], V1L[1]};
    };
    auto bias_unit = [&](int f, u32x4 iv) {
        lds_ptr T = L + OFF_RPB;
        const u32x4 b0 = lds_r128(T + 16 * iv[0]), b1 = lds_r128(T + 16 * iv[1]), b2 = lds_r128(T + 16 * iv[2]), b3 = lds_r128(T + 16 * iv[3]);
        lds_ptr D = L + OFF_BH + 1024 * f + 16 * lane;
#pragma unroll
        for (int hb = 0; hb < 4; ++hb) lds_w128(D + 9216 * hb, u32x4{b0[hb], b1[hb], b2[hb], b3[hb]});
    };

    {
        lds_ptr X = L + OFF_X + p * XS;
        lds_ptr xl = L + OFF_XL + 192 * (m >> 2) + 16 * g;
        lds_ptr xa = X + 192 * m + xsw;
        Acc A[4];
        asm volatile("s_waitcnt vmcnt(8)" : "+v"(ivA) :: "memory");
        BARRIER();
        {
            uint32_t v0 = xr[0], v1 = xr[1], v2 = xr[2];
            if (p == 1 && ww0 == 28) {
                int l2 = lane;
                asm volatile("" : "+v"(l2));
#pragma unroll
                for (int j = 0; j < 3; ++j) {
                    const uint32_t pos = 144u * h + 64u * j + l2;
                    const uint32_t a = (pos * 781u) >> 16;
                    const uint32_t d = (pos - 84u * a >= 48u) ? (uint32_t)-43008 : 0u;
                    if (j == 0) v0 += d; else if (j == 1) v1 += d; else v2 += d;
                }
            }
            dma144nt(xm + 2688, v0, v1, v2, Lbase + OFF_X + (p + 2) * XS + 2304 * h);
        }
        load_wf(0);
        proj_step(A[3], lds_r128(xl), 0, true);
#pragma unroll
        for (int tt = 0; tt < 3; ++tt) proj_step(A[tt], lds_r128(xa + 3072 * tt), 0, true);
        bias_unit(wv, ivA);
        if (wv == 4) bias_unit(8, ivB);
        else if (wv == 5) {
            const f32x4 b48 = lds_rf128(L + OFF_RPB + 16 * ivB[0]);
            u32x4 pr;
#pragma unroll
            for (int hb = 0; hb < 4; ++hb) {
                const uint32_t hi = pk(b48[hb], 0.f) & 0xffffu;
                pr[hb] = hi | (pk(b48[hb] - bflo(hi), 0.f) << 16);
            }
            const int a48 = 128 * lane + 16 * (7 ^ ((lane >> 1) & 7));
            if (lane < 48) {
#pragma unroll
                for (int i = 0; i < 4; ++i) lds_w128(L + OFF_MASK + i * MS + a48, pr);
            }
        } else if (wv == 6) lds_w128(L + OFF_BL + 16 * lane, lds_r128(L + OFF_RPB + 16 * ivB[0]));
        WAIT_VM(8);
        BARRIER();
        load_wf(1);
        proj_step(A[3], lds_r128(xl + 64), 1, false);
#pragma unroll
        for (int tt = 0; tt < 3; ++tt) proj_step(A[tt], lds_r128(xa + 3072 * tt + 64), 1, false);
        {
            lds_ptr B = L + OFF_BH + 9216 * h + 16 * lane;
#pragma unroll
            for (int qt = 0; qt < 3; ++qt)
#pragma unroll
                for (int kt = 0; kt < 3; ++kt) bias[qt][kt] = lds_rf128(B + 1024 * (3 * qt + kt));
            biasL = lds_rf(L + OFF_BL + 16 * lane + 4 * h);
        }
        WAIT_VM(5);
        BARRIER();
        dma144((const char*)wo + 2304 * wv, 16u * lane, Lbase + OFF_WO + 2304 * wv);
        load_mask(p + 2);
        load_wf(2);
        proj_step(A[3], lds_r128(xl + 128), 2, false);
#pragma unroll
        for (int tt = 0; tt < 3; ++tt) proj_step(A[tt], lds_r128(xa + 3072 * tt + 128), 2, false);
        u32x4 QLr = {0u, 0u, 0u, 0u};
        proj_fin(A[3], QLr, F.K[3], V0L, V1L);
        if ((m & 3) == 0) lds_w128(QL + (m >> 2) * 64 + g * 16, QLr);
#pragma unroll
        for (int tt = 0; tt < 3; ++tt) proj_fin(A[tt], F.Q[tt], F.K[tt], V0[tt], V1[tt]);
        set_vt();
        WAIT_VM(5);
        BARRIER();
    }

    int ln = lane;
    asm volatile("" : "+v"(ln));
    const int g2 = ln >> 4, m2 = ln & 15;
    u32x4 IHL;
    {
        const uint32_t val = (g2 < 2) ? 0x3FB9u : 0xBB2Cu;
        const int j = m2 - 8 * (g2 & 1);
#pragma unroll
        for (int r = 0; r < 4; ++r) IHL[r] = (j == 2 * r) ? val : (j == 2 * r + 1) ? (val << 16) : 0u;
    }
    const int vsw = (m2 >> 1) & 7;
    const int ma0 = 128 * m2 + 16 * ((0 + (g2 & 1)) ^ vsw), ma1 = 128 * m2 + 16 * ((2 + (g2 & 1)) ^ vsw), ma2 = 128 * m2 + 16 * ((4 + (g2 & 1)) ^ vsw);
    const int q3a = 128 * m2 + (g2 < 2 ? 16 * (6 ^ vsw) + 12 : 16 * (7 ^ vsw) + 4 * h);
    const int mLa = 128 * 48 + (ln < 48 ? 2 * ln : 110);
    int qa[4];
#pragma unroll
    for (int kt = 0; kt < 4; ++kt) qa[kt] = ((m2 >> 2) == kt) ? 16 * g2 : (OFF_ZERO - OFF_QL - wv * 256 + 16 * g2);
    int woa0, woa2;
    {
        const int q4 = m2 >> 2, p4 = m2 & 3;
        const int rb = 24 * ((q4 >> 1) ^ (g2 & 1)) + (q4 & 1);
        woa0 = OFF_WO + (rb + 4 * g2) * 192 + 16 * p4;
        woa2 = OFF_WO + (rb + 16 + 2 * g2) * 192 + 16 * p4;
    }
    auto wo_frag = [&](int colb, int s) -> u32x4 {
        lds_ptr a = L + (s == 2 ? woa2 : woa0 + (s == 1 ? 384 : 0)) + colb;
        u32x2 lo = lds_tr(a), hi = lds_tr(a + 48 * 192);
        return u32x4{lo[0], lo[1], hi[0], hi[1]};
    };

#pragma unroll
    for (int it = 0; it < 2; ++it) {
        const int i = p + 2 * it;
        const int ww = ww0 + i;
        lds_ptr M = L + OFF_MASK + i * MS;
        lds_ptr OB = L + OFF_OB + i * OBS;
        lds_ptr OL = L + OFF_OL + p * 3072;
        uint32_t o[4][3];
        const f32x4 CW = {(g2 == i) ? 0.f : NEG_BIG, 0.f, 0.f, 0.f};
        const bool validL = (ln < 48) || (ln == 48 + 4 * i);
        uint32_t sb[4];
        const float mL = bflo(lds_r16(M + mLa));
        const float cL = validL ? __builtin_fmaf(mL, LOG2E, biasL) : NEG_BIG;
        auto check = [&]() -> bool {
            const uint32_t lo = min(min(sb[0], sb[1]), min(sb[2], sb[3])), hi = max(max(sb[0], sb[1]), max(sb[2], sb[3]));
            const bool bad = (lo < 0x2B800000u) || (hi > 0x53800000u);
            return __builtin_amdgcn_ballot_w64(bad) != 0ull;
        };
        {
#pragma unroll
            for (int tt = 0; tt < 3; ++tt) F.Q[tt][3] = lds_r32(M + 2048 * tt + q3a);
            Sc S0, S1, S2;
            left_scores<false>(F, cL, validL, QL + 64 * i, qa, PL);
            scores(F, F.Q[0], bias[0], CW, M, ma0, ma1, ma2, IHL, S0);
            scores(F, F.Q[1], bias[1], CW, M + 2048, ma0, ma1, ma2, IHL, S1);
            exp_pv(F, S0, o[0], sb[0]);
            scores(F, F.Q[2], bias[2], CW, M + 4096, ma0, ma1, ma2, IHL, S2);
            exp_pv(F, S1, o[1], sb[1]);
            left_pv(F, PL, o[3], sb[3]);
            exp_pv(F, S2, o[2], sb[2]);
        }
        if (check()) {
#pragma unroll
            for (int qt = 0; qt < 3; ++qt) {
                Sc S;
                u32x4 Q = F.Q[qt];
                Q[3] = 0u;
                scores_safe(F, Q, bias[qt], CW, M + 2048 * qt, S);
                exp_pv(F, S, o[qt], sb[qt]);
            }
            left_scores<true>(F, cL, validL, QL + 64 * i, qa, PL);
            left_pv(F, PL, o[3], sb[3]);
        }
        {
            const int osw = ln * 16 + 4 * (h ^ (g2 & 1));
#pragma unroll
            for (int qt = 0; qt < 3; ++qt)
#pragma unroll
                for (int s = 0; s < 3; ++s) lds_w32(OB + osw + qt * 3072 + s * 1024, o[qt][s]);
            if (m2 == it) {
#pragma unroll
                for (int s = 0; s < 3; ++s) lds_w32(OL + osw + s * 1024, o[3][s]);
            }
        }
        if (it == 0) WAIT_VM(0);
        arrive(CNT);
        if (it == 0) {
            lds_ptr X = L + OFF_X + (p + 2) * XS + xsw;
#pragma unroll
            for (int tt = 0; tt < 3; ++tt) {
                lds_ptr xa = X + 192 * (16 * tt + m);
                u32x4 xf0 = lds_r128(xa), xf1 = lds_r128(xa + 64), xf2 = lds_r128(xa + 128);
                Acc A;
                proj_step(A, xf0, 0, true); proj_step(A, xf1, 1, false); proj_step(A, xf2, 2, false);
                proj_fin(A, F.Q[tt], F.K[tt], V0[tt], V1[tt]);
            }
            set_vt();
        }
        wait_cnt(CNT, 4 * (it + 1));
        if (role < 3) {
            const int cb = 64 * role;
            u32x4 w0 = wo_frag(cb, 0), w1 = wo_frag(cb, 1), w2 = wo_frag(cb, 2);
            u32x4 w3 = wo_frag(cb + 8, 0), w4 = wo_frag(cb + 8, 1), w5 = wo_frag(cb + 8, 2);
            uint32_t sx[3];
            const uint32_t org = org0 + 1344u * i;
#pragma unroll
            for (int qt = 0; qt < 3; ++qt) sx[qt] = so[qt] + org;
            if (ww == 31) {
#pragma unroll
                for (int qt = 0; qt < 3; ++qt) {
                    const uint32_t t = 16u * qt + m2, a = (t * 37u) >> 8;
                    sx[qt] += (t - 7u * a >= 4u) ? (uint32_t)-43008 : 0u;
                }
            }
#pragma unroll
            for (int qt = 0; qt < 3; ++qt) {
                lds_ptr orr = OB + qt * 3072 + ln * 16;
                u32x4 of0 = lds_r128(orr), of1 = lds_r128(orr + 1024), of2 = lds_r128(orr + 2048);
                f32x4 c0 = mfma(w0, of0, ZERO), c1 = mfma(w3, of0, ZERO);
                c0 = mfma(w1, of1, c0); c1 = mfma(w4, of1, c1);
                c0 = mfma(w2, of2, c0); c1 = mfma(w5, of2, c1);
                u32x4 r = {pk(c0[0], c0[1]), pk(c0[2], c0[3]), pk(c1[0], c1[1]), pk(c1[2], c1[3])};
                gstore128(out, sx[qt], r);
            }
        } else if (it == 1) {
            lds_ptr orr = OL + ln * 16;
            u32x4 of0 = lds_r128(orr), of1 = lds_r128(orr + 1024), of2 = lds_r128(orr + 2048);
            int y = 7 * wh + 9; if (y >= 224) y -= 224;
            int xx = 7 * (ww0 + p + 2 * (m2 & 1)) + 9; if (xx >= 224) xx -= 224;
            char* dst = (char*)out + (y * 224 + xx) * 192 + 16 * g2;
#pragma unroll
            for (int i2 = 0; i2 < 3; ++i2) {
                f32x4 c0 = mfma(wo_frag(64 * i2, 0), of0, ZERO);
                f32x4 c1 = mfma(wo_frag(64 * i2 + 8, 0), of0, ZERO);
                c0 = mfma(wo_frag(64 * i2, 1), of1, c0);
                c1 = mfma(wo_frag(64 * i2 + 8, 1), of1, c1);
                c0 = mfma(wo_frag(64 * i2, 2), of2, c0);
                c1 = mfma(wo_frag(64 * i2 + 8, 2), of2, c1);
                u32x4 r = {pk(c0[0], c0[1]), pk(c0[2], c0[3]), pk(c1[0], c1[1]), pk(c1[2], c1[3])};
                if (m2 < 2) gstore128(out, (uint32_t)((y * 224 + xx) * 192 + 16 * g2 + 64 * i2), r);
            }
        }
    }
}

extern "C" void kernel_launch(void* const* d_in, const int* in_sizes, int n_in, void* d_out, int out_size, void* d_ws,
                              size_t ws_size, hipStream_t stream) {
    (void)in_sizes; (void)n_in; (void)out_size; (void)d_ws; (void)ws_size;
    (void)hipFuncSetAttribute((const void*)wattn_kernel, hipFuncAttributeMaxDynamicSharedMemorySize, LDS_TOTAL);
    wattn_kernel<<<256, 512, LDS_TOTAL, stream>>>((const uint16_t*)d_in[0], (const uint16_t*)d_in[1], (const uint16_t*)d_in[2],
                                                  (const uint16_t*)d_in[3], (const int*)d_in[4], (const uint16_t*)d_in[5],
                                                  (uint16_t*)d_out);
}
```

```cpp
#include <hip/hip_runtime.h>
#include <hip/hip_bf16.h>
#include <stdint.h>
#include <type_traits>

typedef __attribute__((ext_vector_type(8))) __bf16 bf16x8;
typedef __attribute__((ext_vector_type(2))) __bf16 bf16x2;
typedef __attribute__((ext_vector_type(4))) float f32x4;
typedef __attribute__((ext_vector_type(2))) float f32x2;
typedef __attribute__((ext_vector_type(4))) uint32_t u32x4;
typedef __attribute__((ext_vector_type(2))) uint32_t u32x2;
typedef __attribute__((ext_vector_type(4))) short s16x4;

#define DEV __device__ __forceinline__
#define LAS __attribute__((address_space(3)))
typedef LAS char* lds_ptr;

constexpr int OFF_W = 0;
constexpr int OFF_OB = 0, OBS = 9216;
constexpr int OFF_OL = 36864;
constexpr int OFF_RPB = 55296;
constexpr int OFF_X = 58016, XS = 9216;
constexpr int OFF_XL = OFF_X + 4 * XS;
constexpr int OFF_MASK = (OFF_XL + 768 + 127) / 128 * 128, MS = 6272;
constexpr int OFF_WO = OFF_MASK + 4 * MS;
constexpr int OFF_BH = OFF_WO;
constexpr int OFF_QL = OFF_BH + 36864;
constexpr int OFF_PL = OFF_QL + 2048;
constexpr int OFF_BL = OFF_PL + 1024;
constexpr int OFF_CNT = OFF_BL + 1024;
constexpr int LDS_TOTAL = OFF_CNT + 64;
static_assert(LDS_TOTAL <= 163840 - 1024, "LDS");
static_assert(OFF_X % 16 == 0 && OFF_MASK % 128 == 0 && MS % 128 == 0 && OFF_WO % 16 == 0 && OFF_QL % 16 == 0, "align");

constexpr float LOG2E = 1.4426950408889634f;
constexpr float QSCALE = 0.20412414523193154f * 1.4426950408889634f;
constexpr float NEG_BIG = -1.0e30f;

DEV uint32_t pk(float a, float b) {
    f32x2 v = {a, b};
    bf16x2 r = __builtin_convertvector(v, bf16x2);
    return __builtin_bit_cast(uint32_t, r);
}
DEV uint32_t pks(float a, float b, float s) { return pk(a * s, __builtin_fmaf(b, s, 0.0f)); }
DEV f32x4 mfma(u32x4 a, u32x4 b, f32x4 c) {
    return __builtin_amdgcn_mfma_f32_16x16x32_bf16(__builtin_bit_cast(bf16x8, a), __builtin_bit_cast(bf16x8, b), c, 0, 0, 0);
}
DEV u32x4 lds_r128(lds_ptr p) { return *(const LAS u32x4*)p; }
DEV f32x4 lds_rf128(lds_ptr p) { return *(const LAS f32x4*)p; }
DEV u32x2 lds_r64(lds_ptr p) { return *(const LAS u32x2*)p; }
DEV uint32_t lds_r32(lds_ptr p) { return *(const LAS uint32_t*)p; }
DEV float lds_rf(lds_ptr p) { return *(const LAS float*)p; }
DEV uint32_t lds_r16(lds_ptr p) { return (uint32_t) * (const LAS uint16_t*)p; }
DEV void lds_w128(lds_ptr p, u32x4 v) { *(LAS u32x4*)p = v; }
DEV void lds_w32(lds_ptr p, uint32_t v) { *(LAS uint32_t*)p = v; }
DEV void lds_w16(lds_ptr p, uint32_t v) { *(LAS uint16_t*)p = (uint16_t)v; }
DEV u32x2 lds_tr(lds_ptr p) {
    return __builtin_bit_cast(u32x2, __builtin_amdgcn_ds_read_tr16_b64_v4i16((LAS s16x4*)p));
}
DEV float bflo(uint32_t v) { return __builtin_bit_cast(float, v << 16); }
DEV float bfhi(uint32_t v) { return __builtin_bit_cast(float, v & 0xffff0000u); }
DEV float ex2(float v) { return __builtin_amdgcn_exp2f(v); }

DEV void dma16nt(const void* sbase, uint32_t voff, uint32_t lds_dst) {
    unsigned keep;
    asm volatile("s_mov_b32 %0, m0\n\ts_mov_b32 m0, %3\n\ts_nop 0\n\tglobal_load_lds_dwordx4 %1, %2 nt\n\ts_mov_b32 m0, %0"
                 : "=&s"(keep) : "v"(voff), "s"(sbase), "s"(lds_dst) : "memory");
}
DEV void dma144(const void* sbase, uint32_t voff, uint32_t lds_dst) {
    unsigned keep;
    asm volatile("s_mov_b32 %0, m0\n\ts_mov_b32 m0, %3\n\ts_nop 0\n\t"
                 "global_load_lds_dwordx4 %1, %2\n\t"
                 "global_load_lds_dwordx4 %1, %2 offset:1024\n\t"
                 "s_mov_b64 exec, 0xffff\n\t"
                 "global_load_lds_dwordx4 %1, %2 offset:2048\n\t"
                 "s_mov_b64 exec, -1\n\ts_mov_b32 m0, %0"
                 : "=&s"(keep) : "v"(voff), "s"(sbase), "s"(lds_dst) : "memory");
}
DEV void dma144nt(const void* sbase, uint32_t v0, uint32_t v1, uint32_t v2, uint32_t lds_dst) {
    unsigned keep;
    asm volatile("s_mov_b32 %0, m0\n\ts_mov_b32 m0, %5\n\ts_nop 0\n\t"
                 "global_load_lds_dwordx4 %1, %4 nt\n\t"
                 "global_load_lds_dwordx4 %2, %4 offset:1024 nt\n\t"
                 "s_mov_b64 exec, 0xffff\n\t"
                 "global_load_lds_dwordx4 %3, %4 offset:2048 nt\n\t"
                 "s_mov_b64 exec, -1\n\ts_mov_b32 m0, %0"
                 : "=&s"(keep) : "v"(v0), "v"(v1), "v"(v2), "s"(sbase), "s"(lds_dst) : "memory");
}
DEV u32x4 gload128(const void* sbase, uint32_t voff) {
    u32x4 v;
    asm volatile("global_load_dwordx4 %0, %1, %2" : "=v"(v) : "v"(voff), "s"(sbase) : "memory");
    return v;
}
DEV uint32_t gload32(const void* sbase, uint32_t voff) {
    uint32_t v;
    asm volatile("global_load_dword %0, %1, %2" : "=v"(v) : "v"(voff), "s"(sbase) : "memory");
    return v;
}
DEV void gstore128(void* sbase, uint32_t voff, u32x4 v) {
    asm volatile("global_store_dwordx4 %0, %1, %2 sc0 sc1\n\ts_nop 1" :: "v"(voff), "v"(v), "s"(sbase) : "memory");
}
#define BARRIER() do { asm volatile("s_waitcnt lgkmcnt(0)" ::: "memory"); __builtin_amdgcn_s_barrier(); asm volatile("" ::: "memory"); } while (0)
#define WAIT_VM(n) asm volatile("s_waitcnt vmcnt(" #n ")" ::: "memory")

DEV void arrive(uint32_t cnt_addr, uint32_t inc) {
    uint32_t t0, t1;
    asm volatile("v_mov_b32 %0, %2\n\tv_mov_b32 %1, %3\n\ts_mov_b64 exec, 1\n\t"
                 "ds_add_u32 %0, %1\n\ts_mov_b64 exec, -1"
                 : "=&v"(t0), "=&v"(t1) : "s"(cnt_addr), "s"(inc) : "memory");
}
DEV uint32_t peek(lds_ptr cnt) { return __hip_atomic_load((LAS uint32_t*)cnt, __ATOMIC_RELAXED, __HIP_MEMORY_SCOPE_WORKGROUP); }
DEV void wait_fields(lds_ptr cnt, uint32_t v, uint32_t mask, uint32_t target) {
    while (((uint32_t)__builtin_amdgcn_readfirstlane(v) & mask) != target) v = peek(cnt);
    asm volatile("" ::: "memory");
}

struct Frags {
    u32x4 Q[3], K[4];
    u32x4 VT[2][2];
};

DEV void pv_norm(const Frags& F, u32x4 P0, u32x4 P1, uint32_t (&o)[3], uint32_t& sumbits) {
    f32x4 zero = {0.f, 0.f, 0.f, 0.f};
    f32x4 oa = mfma(F.VT[0][0], P0, zero);
    f32x4 ob = mfma(F.VT[1][0], P0, zero);
    oa = mfma(F.VT[0][1], P1, oa);
    ob = mfma(F.VT[1][1], P1, ob);
    float sum = ob[2];
    sumbits = __builtin_bit_cast(uint32_t, sum);
    float rs = __builtin_amdgcn_rcpf(sum);
    o[0] = pks(oa[0], oa[1], rs);
    o[1] = pks(oa[2], oa[3], rs);
    o[2] = pks(ob[0], ob[1], rs);
}

struct Sc { f32x4 s0, s1, s2; float s30; };
DEV void scores(const Frags& F, u32x4 Q, const f32x4 (&bias)[3], f32x4 CW, const u32x4 (&mk)[3], u32x4 IHL, Sc& S) {
    S.s0 = mfma(F.K[0], Q, bias[0]);
    S.s1 = mfma(F.K[1], Q, bias[1]);
    S.s2 = mfma(F.K[2], Q, bias[2]);
    f32x4 s3 = mfma(F.K[3], Q, CW);
    S.s30 = s3[0];
    S.s0 = mfma(IHL, mk[0], S.s0);
    S.s1 = mfma(IHL, mk[1], S.s1);
    S.s2 = mfma(IHL, mk[2], S.s2);
}
DEV void exp_pv(const Frags& F, const Sc& S, uint32_t (&o)[3], uint32_t& sumbits) {
    u32x4 P0 = {pk(ex2(S.s0[0]), ex2(S.s0[1])), pk(ex2(S.s0[2]), ex2(S.s0[3])), pk(ex2(S.s1[0]), ex2(S.s1[1])), pk(ex2(S.s1[2]), ex2(S.s1[3]))};
    u32x4 P1 = {pk(ex2(S.s2[0]), ex2(S.s2[1])), pk(ex2(S.s2[2]), ex2(S.s2[3])), pk(ex2(S.s30), 0.f), 0u};
    pv_norm(F, P0, P1, o, sumbits);
}
DEV void scores_safe(const Frags& F, u32x4 Q, const f32x4 (&bias)[3], f32x4 CW, lds_ptr M, Sc& S) {
    const int lane = threadIdx.x & 63;
    const int g = lane >> 4, mm = lane & 15;
    const int v = (mm >> 1) & 7;
    f32x4 zero = {0.f, 0.f, 0.f, 0.f};
    f32x4 s0 = mfma(F.K[0], Q, bias[0]);
    f32x4 s1 = mfma(F.K[1], Q, bias[1]);
    f32x4 s2 = mfma(F.K[2], Q, bias[2]);
    f32x4 s3 = mfma(F.K[3], Q, zero);
    const int h4 = 4 * (__builtin_amdgcn_readfirstlane(threadIdx.x >> 6) & 3);
    const uint32_t bb = lds_r32(M + 128 * mm + 16 * (7 ^ v) + h4);
    const float m48 = bflo(lds_r16(M + 128 * mm + 16 * (6 ^ v) + 14));
    float s30 = __builtin_fmaf(m48, LOG2E, s3[0] + (bflo(bb) + bfhi(bb)));
    u32x2 mv0 = lds_r64(M + 128 * mm + 16 * ((0 + (g >> 1)) ^ v) + 8 * (g & 1));
    u32x2 mv1 = lds_r64(M + 128 * mm + 16 * ((2 + (g >> 1)) ^ v) + 8 * (g & 1));
    u32x2 mv2 = lds_r64(M + 128 * mm + 16 * ((4 + (g >> 1)) ^ v) + 8 * (g & 1));
#define ADDM(s, mv) s[0] = __builtin_fmaf(bflo(mv[0]), LOG2E, s[0]); s[1] = __builtin_fmaf(bfhi(mv[0]), LOG2E, s[1]); \
                    s[2] = __builtin_fmaf(bflo(mv[1]), LOG2E, s[2]); s[3] = __builtin_fmaf(bfhi(mv[1]), LOG2E, s[3]);
    ADDM(s0, mv0) ADDM(s1, mv1) ADDM(s2, mv2)
#undef ADDM
    s30 = (CW[0] == 0.f) ? s30 : -INFINITY;
    float mx = s30;
#pragma unroll
    for (int r = 0; r < 4; ++r) mx = fmaxf(mx, fmaxf(s0[r], fmaxf(s1[r], s2[r])));
    mx = fmaxf(mx, __shfl_xor(mx, 16));
    mx = fmaxf(mx, __shfl_xor(mx, 32));
#pragma unroll
    for (int r = 0; r < 4; ++r) { s0[r] -= mx; s1[r] -= mx; s2[r] -= mx; }
    S.s0 = s0; S.s1 = s1; S.s2 = s2; S.s30 = s30 - mx;
}

template <bool SAFE>
DEV void left_scores(const Frags& F, float cinit, bool valid, u32x4 ql, lds_ptr PLw) {
    const int g = (threadIdx.x & 63) >> 4;
    const f32x4 c = {cinit, 0.f, 0.f, 0.f};
    const f32x4 s0 = mfma(ql, F.K[0], c), s1 = mfma(ql, F.K[1], c), s2 = mfma(ql, F.K[2], c), s3 = mfma(ql, F.K[3], c);
    const float va = (g & 1) ? s1[0] : s0[0], vb = (g & 1) ? s3[0] : s2[0];
    float v = (g & 2) ? vb : va;
    if (SAFE) {
        v = valid ? v : -INFINITY;
        float mx = v;
#pragma unroll
        for (int d = 1; d < 64; d <<= 1) mx = fmaxf(mx, __shfl_xor(mx, d));
        v -= mx;
    }
    lds_w16(PLw, pk(ex2(v), 0.f));
}
struct PLd { u32x2 a0, a1, a2, a3; };
DEV PLd left_ld(lds_ptr PLr) { return PLd{lds_r64(PLr), lds_r64(PLr + 32), lds_r64(PLr + 64), lds_r64(PLr + 96)}; }
DEV void left_pv(const Frags& F, const PLd& a, uint32_t (&o)[3], uint32_t& sumbits) {
    u32x4 P0 = {a.a0[0], a.a0[1], a.a1[0], a.a1[1]}, P1 = {a.a2[0], a.a2[1], a.a3[0], a.a3[1]};
    pv_norm(F, P0, P1, o, sumbits);
}
#define SCHED_FENCE() __builtin_amdgcn_sched_barrier(0)
#define NPRE0 2

__global__ __launch_bounds__(512, 2) void wattn_kernel(const uint16_t* __restrict__ x, const uint16_t* __restrict__ wqkv,
                                                       const uint16_t* __restrict__ wo, const uint16_t* __restrict__ rpb,
                                                       const int* __restrict__ rpidx, const uint16_t* __restrict__ mask,
                                                       uint16_t* __restrict__ out) {
    extern __shared__ __attribute__((aligned(16))) char smem_[];
    lds_ptr L = (lds_ptr)smem_;
    const uint32_t Lbase = (uint32_t)(uintptr_t)L;
    const int tid = threadIdx.x, lane = tid & 63;
    const int wv = __builtin_amdgcn_readfirstlane(tid >> 6);
    const int h = wv & 3, p = wv >> 2;
    const int g = lane >> 4, m = lane & 15;
    const int blk = blockIdx.x;
    const int wh = blk >> 3, ww0 = (blk & 7) * 4;
    const uint32_t wrapY = (wh == 31) ? (uint32_t)-9633792 : 0u;
    const uint32_t org0 = (uint32_t)(((7 * wh + 3) * 224 + 7 * ww0 + 3) * 192);
    const int role = (h + 2 * p) & 3;

    const uint32_t rsrc = 4 * (tid < 338 ? tid : 337);
    const uint32_t l48 = lane < 48 ? lane : 48;
    const uint32_t ilo = 196u * m + 16u * g;
    const int fq = (wv * 11) >> 5;
    const uint32_t isrcA = ilo + (uint32_t)(3136 * fq + 64 * (wv - 3 * fq));
    const uint32_t isrcB = (wv == 4) ? ilo + 6400u : (wv == 5) ? 196u * l48 + 192u : 9408u + 4u * l48;
    uint32_t xr[3];
#pragma unroll
    for (int j = 0; j < 3; ++j) {
        const uint32_t pos = 144u * h + 64u * j + lane;
        const uint32_t a = (pos * 781u) >> 16, p96 = (pos * 683u) >> 16;
        xr[j] = ((pos << 4) ^ ((p96 & 1u) << 5)) + 41664u * a + (pos >= 336u ? wrapY : 0u) + (org0 + 1344u * p + 8192u - 1024u * j);
    }
    const uint32_t wvoff = (16u * lane) ^ (uint32_t)(32 * ((wv >> 1) & 1));
    uint32_t msrc[2];
    bool mok[2];
#pragma unroll
    for (int j = 0; j < 2; ++j) {
        const int pos = 98 * h + 64 * j + lane;
        const int q = pos >> 3, mk = (pos & 7) ^ ((q >> 1) & 7);
        msrc[j] = 98 * q + (mk < 6 ? 16 * mk : 82);
        mok[j] = (mk < 7) && (j == 0 || lane < 34);
    }
    if (wv == 7 && lane == 0) lds_w32(L + OFF_CNT, 0u);
    asm volatile("" :: "v"(rsrc), "v"(isrcA), "v"(isrcB), "v"(xr[0]), "v"(xr[1]), "v"(xr[2]), "v"(wvoff), "v"(msrc[0]), "v"(msrc[1]));
    const float onec = ((m & 3) >= 2) ? 1.0f : 0.0f;
    const f32x4 ONES = {onec, onec, onec, onec};
    const f32x4 ZERO = {0.f, 0.f, 0.f, 0.f};
    const float qsc = (g & 1) ? 1.0f : QSCALE;
    lds_ptr QL = L + OFF_QL + wv * 256;
    lds_ptr CNT = L + OFF_CNT;
    const uint32_t cnt_addr = Lbase + OFF_CNT;
    lds_ptr wab, wc, wv1;
    {
        const int q4 = m >> 2, p4 = m & 3, sx = 32 * (g & 1);
        lds_ptr rb = L + OFF_W + 576 * (8 * g + q4);
        wab = rb + ((48 * h + 8 * p4) ^ sx);
        wc = rb + ((48 * h + 32 + 8 * (p4 >> 1)) ^ sx) + 192 * (p4 & 1);
        wv1 = rb + 384 + ((48 * h + 32 + 8 * (p4 & 1)) ^ sx);
    }
    const int bperm = 4 * (16 * g + 2 * (m >> 2) + (m & 1));
    const uint32_t bmask = ((m & 3) < 2) ? 0xffffffffu : 0u;
    u32x4 IHL;
    {
        const uint32_t val = (g < 2) ? 0x3FB9u : 0xBB2Cu;
        const int j = m - 8 * (g & 1);
#pragma unroll
        for (int r = 0; r < 4; ++r) IHL[r] = (j == 2 * r) ? val : (j == 2 * r + 1) ? (val << 16) : 0u;
    }
    lds_ptr q3a, mLa, qa, osw, oLw, xa, woa0, woa2, PLw, PLr;
    uint32_t ma0;
    {
        const int vsw = (m >> 1) & 7;
        lds_ptr M = L + OFF_MASK + p * MS;
        ma0 = Lbase + OFF_MASK + p * MS + 128 * m + 16 * ((g & 1) ^ vsw);
        q3a = M + 128 * m + (g < 2 ? 16 * (6 ^ vsw) + 12 : 16 * (7 ^ vsw) + 4 * h);
        mLa = M + 128 * 48 + (lane < 48 ? 2 * lane : 110);
        qa = QL + 64 * p + 16 * g;
        osw = L + OFF_OB + p * OBS + lane * 16 + 4 * (h ^ (g & 1));
        oLw = L + OFF_OL + p * 3072 + lane * 16 + 4 * (h ^ (g & 1));
        xa = L + OFF_X + p * XS + 192 * m + ((16 * g) ^ (((m >> 3) & 1) << 5));
        const int q4 = m >> 2, p4 = m & 3;
        const int rb = 24 * ((q4 >> 1) ^ (g & 1)) + (q4 & 1);
        const int cb = (role < 3) ? 64 * role : 0;
        woa0 = L + OFF_WO + (rb + 4 * g) * 192 + 16 * p4 + cb;
        woa2 = L + OFF_WO + (rb + 16 + 2 * g) * 192 + 16 * p4 + cb;
        PLw = L + OFF_PL + wv * 128 + 2 * lane;
        PLr = L + OFF_PL + wv * 128 + 8 * g;
    }
    uint32_t so[3];
#pragma unroll
    for (int qt = 0; qt < 3; ++qt) {
        const uint32_t t = 16u * qt + m, a = (t * 37u) >> 8;
        so[qt] = 192u * t + 41664u * a + (a >= 4u ? wrapY : 0u) + 16u * g + (uint32_t)(64 * role) + (org0 + 1344u * p);
    }
    asm volatile("" : "+v"(so[0]), "+v"(so[1]), "+v"(so[2]), "+v"(IHL), "+v"(ma0), "+v"(q3a), "+v"(mLa));
    asm volatile("" : "+v"(qa), "+v"(osw), "+v"(xa), "+v"(woa0), "+v"(woa2));
    asm volatile("" : "+v"(wab), "+v"(wc), "+v"(wv1));

    const char* xm = (const char*)x - 8192;
    auto load_w = [&](int s) {
        dma144((const char*)wqkv + 18432 * s + 2304 * wv, wvoff, Lbase + OFF_W + 18432 * s + 2304 * wv);
    };
    auto load_mask = [&](int i) {
        const char* mb = (const char*)mask + (size_t)(4 * blk + i) * 4802;
        const uint32_t dst = Lbase + OFF_MASK + i * MS + 98 * 16 * h;
        if (mok[0]) dma16nt(mb, msrc[0], dst);
        if (mok[1]) dma16nt(mb, msrc[1], dst + 1024);
    };
    u32x4 ivA, ivB = {0u, 0u, 0u, 0u};
    if (wv == 4) ivB = gload128(rpidx, isrcB);
    else if (wv == 5 || wv == 6) ivB[0] = gload32(rpidx, isrcB);
    uint32_t rpbv = gload32(rpb, rsrc);
    ivA = gload128(rpidx, isrcA);
    {
        int y = 7 * wh + 9; if (y >= 224) y -= 224;
        int xx = 7 * (ww0 + (wv >> 1)) + 9; if (xx >= 224) xx -= 224;
        if (lane < 6) dma16nt(x, (uint32_t)((y * 224 + xx) * 192 + 96 * (wv & 1) + 16 * lane), Lbase + OFF_XL + 96 * wv);
    }
    dma144nt(xm, xr[0], xr[1], xr[2], Lbase + OFF_X + p * XS + 2304 * h);
    load_w(0);
    load_w(1);

    asm volatile("s_waitcnt vmcnt(11)" : "+v"(rpbv), "+v"(ivB) :: "memory");
    if (tid < 338) *(LAS f32x2*)(L + OFF_RPB + 8 * tid) = f32x2{bflo(rpbv) * LOG2E, bfhi(rpbv) * LOG2E};
    BARRIER();
    load_w(2);
    auto bias_unit = [&](int f, u32x4 iv) {
        lds_ptr T = L + OFF_RPB;
        const u32x4 b0 = lds_r128(T + 16 * iv[0]), b1 = lds_r128(T + 16 * iv[1]), b2 = lds_r128(T + 16 * iv[2]), b3 = lds_r128(T + 16 * iv[3]);
        lds_ptr D = L + OFF_BH + 1024 * f + 16 * lane;
#pragma unroll
        for (int hb = 0; hb < 4; ++hb) lds_w128(D + 9216 * hb, u32x4{b0[hb], b1[hb], b2[hb], b3[hb]});
    };
    asm volatile("s_waitcnt vmcnt(13)" : "+v"(ivA) :: "memory");
    bias_unit(wv, ivA);
    if (wv == 4) bias_unit(8, ivB);
    else if (wv == 5) {
        const f32x4 b48 = lds_rf128(L + OFF_RPB + 16 * ivB[0]);
        u32x4 pr;
#pragma unroll
        for (int hb = 0; hb < 4; ++hb) {
            const uint32_t hi = pk(b48[hb], 0.f) & 0xffffu;
            pr[hb] = hi | (pk(b48[hb] - bflo(hi), 0.f) << 16);
        }
        const int a48 = 128 * lane + 16 * (7 ^ ((lane >> 1) & 7));
        if (lane < 48) {
#pragma unroll
            for (int i = 0; i < 4; ++i) lds_w128(L + OFF_MASK + i * MS + a48, pr);
        }
    } else if (wv == 6) lds_w128(L + OFF_BL + 16 * lane, lds_r128(L + OFF_RPB + 16 * ivB[0]));
    WAIT_VM(0);
    BARRIER();

    u32x4 Wf[5][3];
    auto load_wf = [&](int s) {
        auto fr = [&](lds_ptr a) -> u32x4 {
            u32x2 lo = lds_tr(a + 18432 * s), hi = lds_tr(a + 18432 * s + 2304);
            return u32x4{lo[0], lo[1], hi[0], hi[1]};
        };
        Wf[0][s] = fr(wab);
        Wf[1][s] = fr(wab + 192);
        Wf[2][s] = fr(wc);
        Wf[3][s] = fr(wab + 384);
        const u32x4 t = fr(wv1);
#pragma unroll
        for (int r = 0; r < 4; ++r) Wf[4][s][r] = (uint32_t)__builtin_amdgcn_ds_bpermute(bperm, (int)t[r]) & bmask;
    };
    struct Acc { f32x4 a0, a1, a2, v0, v1; };
    auto proj_step = [&](Acc& A, u32x4 xf, int s, bool first) {
        A.a0 = mfma(Wf[0][s], xf, first ? ZERO : A.a0);
        A.a1 = mfma(Wf[1][s], xf, first ? ZERO : A.a1);
        A.a2 = mfma(Wf[2][s], xf, first ? ZERO : A.a2);
        A.v0 = mfma(xf, Wf[3][s], first ? ZERO : A.v0);
        A.v1 = mfma(xf, Wf[4][s], first ? ONES : A.v1);
    };
    auto proj_fin = [&](const Acc& A, u32x4& Q, u32x4& K, u32x2& V0, u32x2& V1) {
        const uint32_t c0 = pks(A.a2[0], A.a2[1], qsc), c1 = pks(A.a2[2], A.a2[3], qsc);
        const auto sw = __builtin_amdgcn_permlane16_swap(c0, c1, false, false);
        Q[0] = pks(A.a0[0], A.a0[1], QSCALE); Q[1] = pks(A.a0[2], A.a0[3], QSCALE); Q[2] = sw[0];
        K[0] = pk(A.a1[0], A.a1[1]); K[1] = pk(A.a1[2], A.a1[3]); K[2] = sw[1];
        V0 = u32x2{pk(A.v0[0], A.v0[1]), pk(A.v0[2], A.v0[3])};
        V1 = u32x2{pk(A.v1[0], A.v1[1]), pk(A.v1[2], A.v1[3])};
    };
    Frags F;
    F.Q[0][3] = F.Q[1][3] = F.Q[2][3] = 0u;
    F.K[0][3] = F.K[1][3] = F.K[2][3] = 0u;
    F.K[3][3] = (g == 0) ? 0x3FB90000u : (g == 1) ? 0xBB2C0000u : (g == 2) ? 0x3F803F80u : 0u;
    u32x2 V0L, V1L;
    f32x4 bias[3][3];
    float biasL;
    u32x2 V0[3], V1[3];
    auto set_vt = [&]() {
        F.VT[0][0] = u32x4{V0[0][0], V0[0][1], V0[1][0], V0[1][1]};
        F.VT[0][1] = u32x4{V0[2][0], V0[2][1], V0L[0], V0L[1]};
        F.VT[1][0] = u32x4{V1[0][0], V1[0][1], V1[1][0], V1[1][1]};
        F.VT[1][1] = u32x4{V1[2][0], V1[2][1], V1L[0], V1L[1]};
    };
    auto proj_tile = [&](lds_ptr a, u32x4& Q, u32x4& K, u32x2& v0, u32x2& v1) {
        u32x4 xf0 = lds_r128(a), xf1 = lds_r128(a + 64), xf2 = lds_r128(a + 128);
        Acc A;
        proj_step(A, xf0, 0, true); proj_step(A, xf1, 1, false); proj_step(A, xf2, 2, false);
        proj_fin(A, Q, K, v0, v1);
    };

    {
        lds_ptr xl = L + OFF_XL + 192 * (m >> 2) + 16 * g;
        const u32x4 xl0 = lds_r128(xl), xl1 = lds_r128(xl + 64), xl2 = lds_r128(xl + 128);
        load_wf(0); load_wf(1); load_wf(2);
        const u32x4 x00 = lds_r128(xa), x01 = lds_r128(xa + 64), x02 = lds_r128(xa + 128);
        {
            lds_ptr B = L + OFF_BH + 9216 * h + 16 * lane;
#pragma unroll
            for (int qt = 0; qt < 3; ++qt)
#pragma unroll
                for (int kt = 0; kt < 3; ++kt) bias[qt][kt] = lds_rf128(B + 1024 * (3 * qt + kt));
            biasL = lds_rf(L + OFF_BL + 16 * lane + 4 * h);
        }
        load_mask(p);
        {
            uint32_t v0 = xr[0], v1 = xr[1], v2 = xr[2];
            if (p == 1 && ww0 == 28) {
    #pragma unroll
                for (int j = 0; j < 3; ++j) {
                    const uint32_t pos = 144u * h + 64u * j + lane;
                    const uint32_t a = (pos * 781u) >> 16;
                    const uint32_t d = (pos - 84u * a >= 48u) ? (uint32_t)-43008 : 0u;
                    if (j == 0) v0 += d; else if (j == 1) v1 += d; else v2 += d;
                }
            }
            dma144nt(xm + 2688, v0, v1, v2, Lbase + OFF_X + (p + 2) * XS + 2304 * h);
        }
        load_mask(p + 2);
        Acc A, B;
        proj_step(A, xl0, 0, true); proj_step(A, xl1, 1, false); proj_step(A, xl2, 2, false);
        arrive(cnt_addr, 1u);
        proj_step(B, x00, 0, true); proj_step(B, x01, 1, false); proj_step(B, x02, 2, false);
        u32x4 QLr = {0u, 0u, 0u, 0u};
        proj_fin(A, QLr, F.K[3], V0L, V1L);
        if ((m & 3) == 0) lds_w128(QL + (m >> 2) * 64 + g * 16, QLr);
        proj_fin(B, F.Q[0], F.K[0], V0[0], V1[0]);
        proj_tile(xa + 3072, F.Q[1], F.K[1], V0[1], V1[1]);
        proj_tile(xa + 6144, F.Q[2], F.K[2], V0[2], V1[2]);
        set_vt();
    }
    WAIT_VM(2);
    arrive(cnt_addr, 16u << (4 * p));
    wait_fields(CNT, peek(CNT), 0xFu | (0xF0u << (4 * p)), 8u | (0x40u << (4 * p)));

    auto wo_frag = [&](int colb, int s) -> u32x4 {
        lds_ptr a = (s == 2 ? woa2 : woa0 + (s == 1 ? 384 : 0)) + colb;
        u32x2 lo = lds_tr(a), hi = lds_tr(a + 48 * 192);
        return u32x4{lo[0], lo[1], hi[0], hi[1]};
    };
    struct Pre { u32x4 ql, mk[3][3]; uint32_t q3[3], mL; };
    auto fetch = [&](auto itc, Pre& P) {
        constexpr int it = decltype(itc)::value;
        constexpr int MO = 2 * MS * it;
        P.ql = lds_r128(qa + 128 * it);
        P.mL = lds_r16(mLa + MO);
        uint32_t a0 = ma0;
        asm volatile("" : "+v"(a0));
        lds_ptr ma0p = (lds_ptr)(uintptr_t)a0, ma1p = (lds_ptr)(uintptr_t)(a0 ^ 32u), ma2p = (lds_ptr)(uintptr_t)(a0 ^ 64u);
#pragma unroll
        for (int tt = 0; tt < 3; ++tt) P.q3[tt] = lds_r32(q3a + MO + 2048 * tt);
#pragma unroll
        for (int tt = 0; tt < (it == 0 ? NPRE0 : 3); ++tt) {
            P.mk[tt][0] = lds_r128(ma0p + MO + 2048 * tt);
            P.mk[tt][1] = lds_r128(ma1p + MO + 2048 * tt);
            P.mk[tt][2] = lds_r128(ma2p + MO + 2048 * tt);
        }
    };
    auto window = [&](auto itc, Pre& P) {
        constexpr int it = decltype(itc)::value;
        constexpr int OO = 2 * OBS * it;
        const int i = p + 2 * it;
        uint32_t o[4][3];
        const f32x4 CW = {(g == i) ? 0.f : NEG_BIG, 0.f, 0.f, 0.f};
        const bool validL = (lane < 48) || (lane == 48 + 4 * i);
        uint32_t sb[4];
        asm volatile("" : "+v"(P.mL));
        const float cL = validL ? __builtin_fmaf(bflo(P.mL), LOG2E, biasL) : NEG_BIG;
        auto check = [&]() -> bool {
            const uint32_t lo = min(min(sb[0], sb[1]), min(sb[2], sb[3])), hi = max(max(sb[0], sb[1]), max(sb[2], sb[3]));
            const bool bad = (lo < 0x2B800000u) || (hi > 0x53800000u);
            return __builtin_amdgcn_ballot_w64(bad) != 0ull;
        };
        {
#pragma unroll
            for (int tt = 0; tt < 3; ++tt) F.Q[tt][3] = P.q3[tt];
            Sc S0, S1, S2;
            left_scores<false>(F, cL, validL, P.ql, PLw);
            const PLd pl = left_ld(PLr);
            scores(F, F.Q[0], bias[0], CW, P.mk[0], IHL, S0);
            if (it == 0) {
                uint32_t a0 = ma0;
                asm volatile("" : "+v"(a0));
                lds_ptr ma0p = (lds_ptr)(uintptr_t)a0, ma1p = (lds_ptr)(uintptr_t)(a0 ^ 32u), ma2p = (lds_ptr)(uintptr_t)(a0 ^ 64u);
#pragma unroll
                for (int tt = NPRE0; tt < 3; ++tt) {
                    P.mk[tt][0] = lds_r128(ma0p + 2048 * tt);
                    P.mk[tt][1] = lds_r128(ma1p + 2048 * tt);
                    P.mk[tt][2] = lds_r128(ma2p + 2048 * tt);
                }
            }
            SCHED_FENCE();
            scores(F, F.Q[1], bias[1], CW, P.mk[1], IHL, S1);
            exp_pv(F, S0, o[0], sb[0]);
            scores(F, F.Q[2], bias[2], CW, P.mk[2], IHL, S2);
            exp_pv(F, S1, o[1], sb[1]);
            left_pv(F, pl, o[3], sb[3]);
            exp_pv(F, S2, o[2], sb[2]);
        }
        if (__builtin_expect(check(), 0)) {
            lds_ptr M = L + OFF_MASK + i * MS;
#pragma unroll
            for (int qt = 0; qt < 3; ++qt) {
                Sc S;
                u32x4 Q = F.Q[qt];
                Q[3] = 0u;
                scores_safe(F, Q, bias[qt], CW, M + 2048 * qt, S);
                exp_pv(F, S, o[qt], sb[qt]);
            }
            left_scores<true>(F, cL, validL, lds_r128(qa + 128 * it), PLw);
            left_pv(F, left_ld(PLr), o[3], sb[3]);
        }
#pragma unroll
        for (int qt = 0; qt < 3; ++qt)
#pragma unroll
            for (int s = 0; s < 3; ++s) lds_w32(osw + OO + qt * 3072 + s * 1024, o[qt][s]);
        if (m == it) {
#pragma unroll
            for (int s = 0; s < 3; ++s) lds_w32(oLw + s * 1024, o[3][s]);
        }
        if (it == 0) WAIT_VM(0);
        arrive(cnt_addr, (it == 0 ? 0x1000u : 0x100000u) << (4 * p));
    };
    auto out_store = [&](const f32x4& c0, const f32x4& c1, uint32_t off) {
        u32x4 r = {pk(c0[0], c0[1]), pk(c0[2], c0[3]), pk(c1[0], c1[1]), pk(c1[2], c1[3])};
        gstore128(out, off, r);
    };

    Pre P;
    fetch(std::integral_constant<int, 0>{}, P);
    SCHED_FENCE();
    if (p == 0) {
        dma144((const char*)wo + 4608 * h, 16u * lane, Lbase + OFF_WO + 4608 * h);
        dma144((const char*)wo + 4608 * h + 2304, 16u * lane, Lbase + OFF_WO + 4608 * h + 2304);
    }
    window(std::integral_constant<int, 0>{}, P);
    proj_tile(xa + 2 * XS, F.Q[0], F.K[0], V0[0], V1[0]);
    proj_tile(xa + 2 * XS + 3072, F.Q[1], F.K[1], V0[1], V1[1]);
    proj_tile(xa + 2 * XS + 6144, F.Q[2], F.K[2], V0[2], V1[2]);
    set_vt();
    wait_fields(CNT, peek(CNT), p ? 0xFF000u : 0xF000u, p ? 0x44000u : 0x4000u);
    lds_ptr ord = (lds_ptr)(uintptr_t)((uint32_t)(uintptr_t)osw & ~15u);
    {
        u32x4 of[3][3], w[6];
        if (role < 3) {
#pragma unroll
            for (int s = 0; s < 3; ++s) {
                w[s] = wo_frag(0, s); w[3 + s] = wo_frag(8, s);
#pragma unroll
                for (int qt = 0; qt < 3; ++qt) of[qt][s] = lds_r128(ord + qt * 3072 + s * 1024);
            }
        }
        fetch(std::integral_constant<int, 1>{}, P);
        SCHED_FENCE();
        if (role < 3) {
            f32x4 c0[3], c1[3];
#pragma unroll
            for (int s = 0; s < 3; ++s)
#pragma unroll
                for (int qt = 0; qt < 3; ++qt) {
                    c0[qt] = mfma(w[s], of[qt][s], s ? c0[qt] : ZERO);
                    c1[qt] = mfma(w[3 + s], of[qt][s], s ? c1[qt] : ZERO);
                }
#pragma unroll
            for (int qt = 0; qt < 3; ++qt) out_store(c0[qt], c1[qt], so[qt]);
        }
    }
    window(std::integral_constant<int, 1>{}, P);
    if (role < 3) {
        uint32_t sx[3];
#pragma unroll
        for (int qt = 0; qt < 3; ++qt) sx[qt] = so[qt] + 2688u;
        if (ww0 + p + 2 == 31) {
            uint32_t mo = lane;
            asm volatile("" : "+v"(mo));
            mo &= 15u;
#pragma unroll
            for (int qt = 0; qt < 3; ++qt) {
                const uint32_t t = 16u * qt + mo, a = (t * 37u) >> 8;
                sx[qt] += (t - 7u * a >= 4u) ? (uint32_t)-43008 : 0u;
            }
        }
        u32x4 w[6];
#pragma unroll
        for (int s = 0; s < 3; ++s) { w[s] = wo_frag(0, s); w[3 + s] = wo_frag(8, s); }
        wait_fields(CNT, peek(CNT), 0xF00000u << (4 * p), 0x400000u << (4 * p));
        u32x4 of[3][3];
#pragma unroll
        for (int s = 0; s < 3; ++s)
#pragma unroll
            for (int qt = 0; qt < 3; ++qt) of[qt][s] = lds_r128(ord + 2 * OBS + qt * 3072 + s * 1024);
        f32x4 c0[3], c1[3];
#pragma unroll
        for (int s = 0; s < 3; ++s)
#pragma unroll
            for (int qt = 0; qt < 3; ++qt) {
                c0[qt] = mfma(w[s], of[qt][s], s ? c0[qt] : ZERO);
                c1[qt] = mfma(w[3 + s], of[qt][s], s ? c1[qt] : ZERO);
            }
#pragma unroll
        for (int qt = 0; qt < 3; ++qt) out_store(c0[qt], c1[qt], sx[qt]);
    } else {
        u32x4 w[3][6];
#pragma unroll
        for (int i2 = 0; i2 < 3; ++i2)
#pragma unroll
            for (int s = 0; s < 3; ++s) { w[i2][s] = wo_frag(64 * i2, s); w[i2][3 + s] = wo_frag(64 * i2 + 8, s); }
        int y = 7 * wh + 9; if (y >= 224) y -= 224;
        int xx = 7 * (ww0 + p + 2 * (m & 1)) + 9; if (xx >= 224) xx -= 224;
        const uint32_t dst = (uint32_t)((y * 224 + xx) * 192 + 16 * g);
        wait_fields(CNT, peek(CNT), 0xF00000u << (4 * p), 0x400000u << (4 * p));
        lds_ptr orr = L + OFF_OL + p * 3072 + lane * 16;
        u32x4 of0 = lds_r128(orr), of1 = lds_r128(orr + 1024), of2 = lds_r128(orr + 2048);
        f32x4 c0[3], c1[3];
#pragma unroll
        for (int i2 = 0; i2 < 3; ++i2) { c0[i2] = mfma(w[i2][0], of0, ZERO); c1[i2] = mfma(w[i2][3], of0, ZERO); }
#pragma unroll
        for (int i2 = 0; i2 < 3; ++i2) { c0[i2] = mfma(w[i2][1], of1, c0[i2]); c1[i2] = mfma(w[i2][4], of1, c1[i2]); }
#pragma unroll
        for (int i2 = 0; i2 < 3; ++i2) { c0[i2] = mfma(w[i2][2], of2, c0[i2]); c1[i2] = mfma(w[i2][5], of2, c1[i2]); }
        if (m < 2) {
#pragma unroll
            for (int i2 = 0; i2 < 3; ++i2) out_store(c0[i2], c1[i2], dst + 64 * i2);
        }
    }
}

extern "C" void kernel_launch(void* const* d_in, const int* in_sizes, int n_in, void* d_out, int out_size, void* d_ws,
                              size_t ws_size, hipStream_t stream) {
    (void)in_sizes; (void)n_in; (void)out_size; (void)d_ws; (void)ws_size;
    (void)hipFuncSetAttribute((const void*)wattn_kernel, hipFuncAttributeMaxDynamicSharedMemorySize, LDS_TOTAL);
    wattn_kernel<<<256, 512, LDS_TOTAL, stream>>>((const uint16_t*)d_in[0], (const uint16_t*)d_in[1], (const uint16_t*)d_in[2],
                                                  (const uint16_t*)d_in[3], (const int*)d_in[4], (const uint16_t*)d_in[5],
                                                  (uint16_t*)d_out);
}
```

```cpp
#include <hip/hip_runtime.h>
#include <hip/hip_bf16.h>
#include <stdint.h>
#include <type_traits>

typedef __attribute__((ext_vector_type(8))) __bf16 bf16x8;
typedef __attribute__((ext_vector_type(2))) __bf16 bf16x2;
typedef __attribute__((ext_vector_type(4))) float f32x4;
typedef __attribute__((ext_vector_type(2))) float f32x2;
typedef __attribute__((ext_vector_type(4))) uint32_t u32x4;
typedef __attribute__((ext_vector_type(2))) uint32_t u32x2;
typedef __attribute__((ext_vector_type(4))) short s16x4;

#define DEV __device__ __forceinline__
#define LAS __attribute__((address_space(3)))
typedef LAS char* lds_ptr;

constexpr int OFF_W = 0;
constexpr int OFF_OB = 0, OBS = 9216;
constexpr int OFF_OL = 36864;
constexpr int OFF_RPB = 55296;
constexpr int OFF_X = 58016, XS = 9216;
constexpr int OFF_XL = OFF_X + 4 * XS;
constexpr int OFF_MASK = (OFF_XL + 768 + 127) / 128 * 128, MS = 6272;
constexpr int OFF_WO = OFF_MASK + 4 * MS;
constexpr int OFF_BH = OFF_WO;
constexpr int OFF_QL = OFF_BH + 36864;
constexpr int OFF_PL = OFF_QL + 2048;
constexpr int OFF_BL = OFF_PL + 1024;
constexpr int OFF_CNT = OFF_BL + 1024;
constexpr int LDS_TOTAL = OFF_CNT + 64;
static_assert(LDS_TOTAL <= 163840 - 1024, "LDS");
static_assert(OFF_X % 16 == 0 && OFF_MASK % 128 == 0 && MS % 128 == 0 && OFF_WO % 16 == 0 && OFF_QL % 16 == 0, "align");

constexpr float LOG2E = 1.4426950408889634f;
constexpr float QSCALE = 0.20412414523193154f * 1.4426950408889634f;
constexpr float NEG_BIG = -1.0e30f;

DEV uint32_t pk(float a, float b) {
    f32x2 v = {a, b};
    bf16x2 r = __builtin_convertvector(v, bf16x2);
    return __builtin_bit_cast(uint32_t, r);
}
DEV uint32_t pks(float a, float b, float s) { return pk(a * s, __builtin_fmaf(b, s, 0.0f)); }
DEV f32x4 mfma(u32x4 a, u32x4 b, f32x4 c) {
    return __builtin_amdgcn_mfma_f32_16x16x32_bf16(__builtin_bit_cast(bf16x8, a), __builtin_bit_cast(bf16x8, b), c, 0, 0, 0);
}
DEV u32x4 lds_r128(lds_ptr p) { return *(const LAS u32x4*)p; }
DEV f32x4 lds_rf128(lds_ptr p) { return *(const LAS f32x4*)p; }
DEV u32x2 lds_r64(lds_ptr p) { return *(const LAS u32x2*)p; }
DEV uint32_t lds_r32(lds_ptr p) { return *(const LAS uint32_t*)p; }
DEV float lds_rf(lds_ptr p) { return *(const LAS float*)p; }
DEV uint32_t lds_r16(lds_ptr p) { return (uint32_t) * (const LAS uint16_t*)p; }
DEV void lds_w128(lds_ptr p, u32x4 v) { *(LAS u32x4*)p = v; }
DEV void lds_w32(lds_ptr p, uint32_t v) { *(LAS uint32_t*)p = v; }
DEV void lds_w16(lds_ptr p, uint32_t v) { *(LAS uint16_t*)p = (uint16_t)v; }
DEV u32x2 lds_tr(lds_ptr p) {
    return __builtin_bit_cast(u32x2, __builtin_amdgcn_ds_read_tr16_b64_v4i16((LAS s16x4*)p));
}
DEV float bflo(uint32_t v) { return __builtin_bit_cast(float, v << 16); }
DEV float bfhi(uint32_t v) { return __builtin_bit_cast(float, v & 0xffff0000u); }
DEV float ex2(float v) { return __builtin_amdgcn_exp2f(v); }

DEV void dma16nt(const void* sbase, uint32_t voff, uint32_t lds_dst) {
    unsigned keep;
    asm volatile("s_mov_b32 %0, m0\n\ts_mov_b32 m0, %3\n\ts_nop 0\n\tglobal_load_lds_dwordx4 %1, %2 nt\n\ts_mov_b32 m0, %0"
                 : "=&s"(keep) : "v"(voff), "s"(sbase), "s"(lds_dst) : "memory");
}
DEV void dma144(const void* sbase, uint32_t voff, uint32_t lds_dst) {
    unsigned keep;
    asm volatile("s_mov_b32 %0, m0\n\ts_mov_b32 m0, %3\n\ts_nop 0\n\t"
                 "global_load_lds_dwordx4 %1, %2\n\t"
                 "global_load_lds_dwordx4 %1, %2 offset:1024\n\t"
                 "s_mov_b64 exec, 0xffff\n\t"
                 "global_load_lds_dwordx4 %1, %2 offset:2048\n\t"
                 "s_mov_b64 exec, -1\n\ts_mov_b32 m0, %0"
                 : "=&s"(keep) : "v"(voff), "s"(sbase), "s"(lds_dst) : "memory");
}
DEV void dma144nt(const void* sbase, uint32_t v0, uint32_t v1, uint32_t v2, uint32_t lds_dst) {
    unsigned keep;
    asm volatile("s_mov_b32 %0, m0\n\ts_mov_b32 m0, %5\n\ts_nop 0\n\t"
                 "global_load_lds_dwordx4 %1, %4 nt\n\t"
                 "global_load_lds_dwordx4 %2, %4 offset:1024 nt\n\t"
                 "s_mov_b64 exec, 0xffff\n\t"
                 "global_load_lds_dwordx4 %3, %4 offset:2048 nt\n\t"
                 "s_mov_b64 exec, -1\n\ts_mov_b32 m0, %0"
                 : "=&s"(keep) : "v"(v0), "v"(v1), "v"(v2), "s"(sbase), "s"(lds_dst) : "memory");
}
DEV u32x4 gload128(const void* sbase, uint32_t voff) {
    u32x4 v;
    asm volatile("global_load_dwordx4 %0, %1, %2" : "=v"(v) : "v"(voff), "s"(sbase) : "memory");
    return v;
}
DEV uint32_t gload32(const void* sbase, uint32_t voff) {
    uint32_t v;
    asm volatile("global_load_dword %0, %1, %2" : "=v"(v) : "v"(voff), "s"(sbase) : "memory");
    return v;
}
DEV void gstore128(void* sbase, uint32_t voff, u32x4 v) {
    asm volatile("global_store_dwordx4 %0, %1, %2 sc0 sc1\n\ts_nop 1" :: "v"(voff), "v"(v), "s"(sbase) : "memory");
}
#define BARRIER() do { asm volatile("s_waitcnt lgkmcnt(0)" ::: "memory"); __builtin_amdgcn_s_barrier(); asm volatile("" ::: "memory"); } while (0)
#define WAIT_VM(n) asm volatile("s_waitcnt vmcnt(" #n ")" ::: "memory")

DEV void arrive(uint32_t cnt_addr, uint32_t inc) {
    uint32_t t0, t1;
    asm volatile("v_mov_b32 %0, %2\n\tv_mov_b32 %1, %3\n\ts_mov_b64 exec, 1\n\t"
                 "ds_add_u32 %0, %1\n\ts_mov_b64 exec, -1"
                 : "=&v"(t0), "=&v"(t1) : "s"(cnt_addr), "s"(inc) : "memory");
}
DEV uint32_t peek(lds_ptr cnt) { return __hip_atomic_load((LAS uint32_t*)cnt, __ATOMIC_RELAXED, __HIP_MEMORY_SCOPE_WORKGROUP); }
DEV void wait_fields(lds_ptr cnt, uint32_t v, uint32_t mask, uint32_t target) {
    while (((uint32_t)__builtin_amdgcn_readfirstlane(v) & mask) != target) v = peek(cnt);
    asm volatile("" ::: "memory");
}

struct Frags {
    u32x4 Q[3], K[4];
    u32x4 VT[2][2];
};

DEV void pv_norm(const Frags& F, u32x4 P0, u32x4 P1, uint32_t (&o)[3], uint32_t& sumbits) {
    f32x4 zero = {0.f, 0.f, 0.f, 0.f};
    f32x4 oa = mfma(F.VT[0][0], P0, zero);
    f32x4 ob = mfma(F.VT[1][0], P0, zero);
    oa = mfma(F.VT[0][1], P1, oa);
    ob = mfma(F.VT[1][1], P1, ob);
    float sum = ob[2];
    sumbits = __builtin_bit_cast(uint32_t, sum);
    float rs = __builtin_amdgcn_rcpf(sum);
    o[0] = pks(oa[0], oa[1], rs);
    o[1] = pks(oa[2], oa[3], rs);
    o[2] = pks(ob[0], ob[1], rs);
}

struct Sc { f32x4 s0, s1, s2; float s30; };
DEV void scores(const Frags& F, u32x4 Q, const f32x4 (&bias)[3], f32x4 CW, const u32x4 (&mk)[3], u32x4 IHL, Sc& S) {
    S.s0 = mfma(F.K[0], Q, bias[0]);
    S.s1 = mfma(F.K[1], Q, bias[1]);
    S.s2 = mfma(F.K[2], Q, bias[2]);
    f32x4 s3 = mfma(F.K[3], Q, CW);
    S.s30 = s3[0];
    S.s0 = mfma(IHL, mk[0], S.s0);
    S.s1 = mfma(IHL, mk[1], S.s1);
    S.s2 = mfma(IHL, mk[2], S.s2);
}
DEV void exp_pv(const Frags& F, const Sc& S, uint32_t (&o)[3], uint32_t& sumbits) {
    u32x4 P0 = {pk(ex2(S.s0[0]), ex2(S.s0[1])), pk(ex2(S.s0[2]), ex2(S.s0[3])), pk(ex2(S.s1[0]), ex2(S.s1[1])), pk(ex2(S.s1[2]), ex2(S.s1[3]))};
    u32x4 P1 = {pk(ex2(S.s2[0]), ex2(S.s2[1])), pk(ex2(S.s2[2]), ex2(S.s2[3])), pk(ex2(S.s30), 0.f), 0u};
    pv_norm(F, P0, P1, o, sumbits);
}
DEV void scores_safe(const Frags& F, u32x4 Q, const f32x4 (&bias)[3], f32x4 CW, lds_ptr M, Sc& S) {
    const int lane = threadIdx.x & 63;
    const int g = lane >> 4, mm = lane & 15;
    const int v = (mm >> 1) & 7;
    f32x4 zero = {0.f, 0.f, 0.f, 0.f};
    f32x4 s0 = mfma(F.K[0], Q, bias[0]);
    f32x4 s1 = mfma(F.K[1], Q, bias[1]);
    f32x4 s2 = mfma(F.K[2], Q, bias[2]);
    f32x4 s3 = mfma(F.K[3], Q, zero);
    const int h4 = 4 * (__builtin_amdgcn_readfirstlane(threadIdx.x >> 6) & 3);
    const uint32_t bb = lds_r32(M + 128 * mm + 16 * (7 ^ v) + h4);
    const float m48 = bflo(lds_r16(M + 128 * mm + 16 * (6 ^ v) + 14));
    float s30 = __builtin_fmaf(m48, LOG2E, s3[0] + (bflo(bb) + bfhi(bb)));
    u32x2 mv0 = lds_r64(M + 128 * mm + 16 * ((0 + (g >> 1)) ^ v) + 8 * (g & 1));
    u32x2 mv1 = lds_r64(M + 128 * mm + 16 * ((2 + (g >> 1)) ^ v) + 8 * (g & 1));
    u32x2 mv2 = lds_r64(M + 128 * mm + 16 * ((4 + (g >> 1)) ^ v) + 8 * (g & 1));
#define ADDM(s, mv) s[0] = __builtin_fmaf(bflo(mv[0]), LOG2E, s[0]); s[1] = __builtin_fmaf(bfhi(mv[0]), LOG2E, s[1]); \
                    s[2] = __builtin_fmaf(bflo(mv[1]), LOG2E, s[2]); s[3] = __builtin_fmaf(bfhi(mv[1]), LOG2E, s[3]);
    ADDM(s0, mv0) ADDM(s1, mv1) ADDM(s2, mv2)
#undef ADDM
    s30 = (CW[0] == 0.f) ? s30 : -INFINITY;
    float mx = s30;
#pragma unroll
    for (int r = 0; r < 4; ++r) mx = fmaxf(mx, fmaxf(s0[r], fmaxf(s1[r], s2[r])));
    mx = fmaxf(mx, __shfl_xor(mx, 16));
    mx = fmaxf(mx, __shfl_xor(mx, 32));
#pragma unroll
    for (int r = 0; r < 4; ++r) { s0[r] -= mx; s1[r] -= mx; s2[r] -= mx; }
    S.s0 = s0; S.s1 = s1; S.s2 = s2; S.s30 = s30 - mx;
}

template <bool SAFE>
DEV void left_scores(const Frags& F, float cinit, bool valid, u32x4 ql, lds_ptr PLw) {
    const int g = (threadIdx.x & 63) >> 4;
    const f32x4 c = {cinit, 0.f, 0.f, 0.f};
    const f32x4 s0 = mfma(ql, F.K[0], c), s1 = mfma(ql, F.K[1], c), s2 = mfma(ql, F.K[2], c), s3 = mfma(ql, F.K[3], c);
    const float va = (g & 1) ? s1[0] : s0[0], vb = (g & 1) ? s3[0] : s2[0];
    float v = (g & 2) ? vb : va;
    if (SAFE) {
        v = valid ? v : -INFINITY;
        float mx = v;
#pragma unroll
        for (int d = 1; d < 64; d <<= 1) mx = fmaxf(mx, __shfl_xor(mx, d));
        v -= mx;
    }
    lds_w16(PLw, pk(ex2(v), 0.f));
}
struct PLd { u32x2 a0, a1, a2, a3; };
DEV PLd left_ld(lds_ptr PLr) { return PLd{lds_r64(PLr), lds_r64(PLr + 32), lds_r64(PLr + 64), lds_r64(PLr + 96)}; }
DEV void left_pv(const Frags& F, const PLd& a, uint32_t (&o)[3], uint32_t& sumbits) {
    u32x4 P0 = {a.a0[0], a.a0[1], a.a1[0], a.a1[1]}, P1 = {a.a2[0], a.a2[1], a.a3[0], a.a3[1]};
    pv_norm(F, P0, P1, o, sumbits);
}
#define PR0 -1
#define PR1 1
#define PR0B -1
#define PR1B 5
#define PRIO_HOOK(k) do { if (PR1 == k) { if (p == 1) __builtin_amdgcn_s_setprio(1); } if (PR1B == k) { if (p == 1) __builtin_amdgcn_s_setprio(0); } \
    if (PR0 == k) { if (p == 0) __builtin_amdgcn_s_setprio(2); } if (PR0B == k) { if (p == 0) __builtin_amdgcn_s_setprio(0); } } while (0)
#define SCHED_FENCE() __builtin_amdgcn_sched_barrier(0)
#define NPRE0 2

__global__ __launch_bounds__(512, 2) void wattn_kernel(const uint16_t* __restrict__ x, const uint16_t* __restrict__ wqkv,
                                                       const uint16_t* __restrict__ wo, const uint16_t* __restrict__ rpb,
                                                       const int* __restrict__ rpidx, const uint16_t* __restrict__ mask,
                                                       uint16_t* __restrict__ out) {
    extern __shared__ __attribute__((aligned(16))) char smem_[];
    lds_ptr L = (lds_ptr)smem_;
    const uint32_t Lbase = (uint32_t)(uintptr_t)L;
    const int tid = threadIdx.x, lane = tid & 63;
    const int wv = __builtin_amdgcn_readfirstlane(tid >> 6);
    const int h = wv & 3, p = wv >> 2;
    const int g = lane >> 4, m = lane & 15;
    const int blk = blockIdx.x;
    const int wh = blk >> 3, ww0 = (blk & 7) * 4;
    const uint32_t wrapY = (wh == 31) ? (uint32_t)-9633792 : 0u;
    const uint32_t org0 = (uint32_t)(((7 * wh + 3) * 224 + 7 * ww0 + 3) * 192);
    const int role = (h + 2 * p) & 3;

    const uint32_t rsrc = 4 * (tid < 338 ? tid : 337);
    const uint32_t l48 = lane < 48 ? lane : 48;
    const uint32_t ilo = 196u * m + 16u * g;
    const int fq = (wv * 11) >> 5;
    const uint32_t isrcA = ilo + (uint32_t)(3136 * fq + 64 * (wv - 3 * fq));
    const uint32_t isrcB = (wv == 4) ? ilo + 6400u : (wv == 5) ? 196u * l48 + 192u : 9408u + 4u * l48;
    uint32_t xr[3];
#pragma unroll
    for (int j = 0; j < 3; ++j) {
        const uint32_t pos = 144u * h + 64u * j + lane;
        const uint32_t a = (pos * 781u) >> 16, p96 = (pos * 683u) >> 16;
        xr[j] = ((pos << 4) ^ ((p96 & 1u) << 5)) + 41664u * a + (pos >= 336u ? wrapY : 0u) + (org0 + 1344u * p + 8192u - 1024u * j);
    }
    const uint32_t wvoff = (16u * lane) ^ (uint32_t)(32 * ((wv >> 1) & 1));
    uint32_t msrc[2];
    bool mok[2];
#pragma unroll
    for (int j = 0; j < 2; ++j) {
        const int pos = 98 * h + 64 * j + lane;
        const int q = pos >> 3, mk = (pos & 7) ^ ((q >> 1) & 7);
        msrc[j] = 98 * q + (mk < 6 ? 16 * mk : 82);
        mok[j] = (mk < 7) && (j == 0 || lane < 34);
    }
    if (wv == 7 && lane == 0) lds_w32(L + OFF_CNT, 0u);
    asm volatile("" :: "v"(rsrc), "v"(isrcA), "v"(isrcB), "v"(xr[0]), "v"(xr[1]), "v"(xr[2]), "v"(wvoff), "v"(msrc[0]), "v"(msrc[1]));
    const float onec = ((m & 3) >= 2) ? 1.0f : 0.0f;
    const f32x4 ONES = {onec, onec, onec, onec};
    const f32x4 ZERO = {0.f, 0.f, 0.f, 0.f};
    const float qsc = (g & 1) ? 1.0f : QSCALE;
    lds_ptr QL = L + OFF_QL + wv * 256;
    lds_ptr CNT = L + OFF_CNT;
    const uint32_t cnt_addr = Lbase + OFF_CNT;
    lds_ptr wab, wc, wv1;
    {
        const int q4 = m >> 2, p4 = m & 3, sx = 32 * (g & 1);
        lds_ptr rb = L + OFF_W + 576 * (8 * g + q4);
        wab = rb + ((48 * h + 8 * p4) ^ sx);
        wc = rb + ((48 * h + 32 + 8 * (p4 >> 1)) ^ sx) + 192 * (p4 & 1);
        wv1 = rb + 384 + ((48 * h + 32 + 8 * (p4 & 1)) ^ sx);
    }
    const int bperm = 4 * (16 * g + 2 * (m >> 2) + (m & 1));
    const uint32_t bmask = ((m & 3) < 2) ? 0xffffffffu : 0u;
    u32x4 IHL;
    {
        const uint32_t val = (g < 2) ? 0x3FB9u : 0xBB2Cu;
        const int j = m - 8 * (g & 1);
#pragma unroll
        for (int r = 0; r < 4; ++r) IHL[r] = (j == 2 * r) ? val : (j == 2 * r + 1) ? (val << 16) : 0u;
    }
    lds_ptr q3a, mLa, qa, osw, oLw, xa, woa0, woa2, PLw, PLr;
    uint32_t ma0;
    {
        const int vsw = (m >> 1) & 7;
        lds_ptr M = L + OFF_MASK + p * MS;
        ma0 = Lbase + OFF_MASK + p * MS + 128 * m + 16 * ((g & 1) ^ vsw);
        q3a = M + 128 * m + (g < 2 ? 16 * (6 ^ vsw) + 12 : 16 * (7 ^ vsw) + 4 * h);
        mLa = M + 128 * 48 + (lane < 48 ? 2 * lane : 110);
        qa = QL + 64 * p + 16 * g;
        osw = L + OFF_OB + p * OBS + lane * 16 + 4 * (h ^ (g & 1));
        oLw = L + OFF_OL + p * 3072 + lane * 16 + 4 * (h ^ (g & 1));
        xa = L + OFF_X + p * XS + 192 * m + ((16 * g) ^ (((m >> 3) & 1) << 5));
        const int q4 = m >> 2, p4 = m & 3;
        const int rb = 24 * ((q4 >> 1) ^ (g & 1)) + (q4 & 1);
        const int cb = (role < 3) ? 64 * role : 0;
        woa0 = L + OFF_WO + (rb + 4 * g) * 192 + 16 * p4 + cb;
        woa2 = L + OFF_WO + (rb + 16 + 2 * g) * 192 + 16 * p4 + cb;
        PLw = L + OFF_PL + wv * 128 + 2 * lane;
        PLr = L + OFF_PL + wv * 128 + 8 * g;
    }
    uint32_t so[3];
#pragma unroll
    for (int qt = 0; qt < 3; ++qt) {
        const uint32_t t = 16u * qt + m, a = (t * 37u) >> 8;
        so[qt] = 192u * t + 41664u * a + (a >= 4u ? wrapY : 0u) + 16u * g + (uint32_t)(64 * role) + (org0 + 1344u * p);
    }
    asm volatile("" : "+v"(so[0]), "+v"(so[1]), "+v"(so[2]), "+v"(IHL), "+v"(ma0), "+v"(q3a), "+v"(mLa));
    asm volatile("" : "+v"(qa), "+v"(osw), "+v"(xa), "+v"(woa0), "+v"(woa2));
    asm volatile("" : "+v"(wab), "+v"(wc), "+v"(wv1));

    const char* xm = (const char*)x - 8192;
    auto load_w = [&](int s) {
        dma144((const char*)wqkv + 18432 * s + 2304 * wv, wvoff, Lbase + OFF_W + 18432 * s + 2304 * wv);
    };
    auto load_mask = [&](int i) {
        const char* mb = (const char*)mask + (size_t)(4 * blk + i) * 4802;
        const uint32_t dst = Lbase + OFF_MASK + i * MS + 98 * 16 * h;
        if (mok[0]) dma16nt(mb, msrc[0], dst);
        if (mok[1]) dma16nt(mb, msrc[1], dst + 1024);
    };
    u32x4 ivA, ivB = {0u, 0u, 0u, 0u};
    if (wv == 4) ivB = gload128(rpidx, isrcB);
    else if (wv == 5 || wv == 6) ivB[0] = gload32(rpidx, isrcB);
    uint32_t rpbv = gload32(rpb, rsrc);
    ivA = gload128(rpidx, isrcA);
    {
        int y = 7 * wh + 9; if (y >= 224) y -= 224;
        int xx = 7 * (ww0 + (wv >> 1)) + 9; if (xx >= 224) xx -= 224;
        if (lane < 6) dma16nt(x, (uint32_t)((y * 224 + xx) * 192 + 96 * (wv & 1) + 16 * lane), Lbase + OFF_XL + 96 * wv);
    }
    dma144nt(xm, xr[0], xr[1], xr[2], Lbase + OFF_X + p * XS + 2304 * h);
    load_w(0);
    load_w(1);

    asm volatile("s_waitcnt vmcnt(11)" : "+v"(rpbv), "+v"(ivB) :: "memory");
    if (tid < 338) *(LAS f32x2*)(L + OFF_RPB + 8 * tid) = f32x2{bflo(rpbv) * LOG2E, bfhi(rpbv) * LOG2E};
    BARRIER();
    load_w(2);
    auto bias_unit = [&](int f, u32x4 iv) {
        lds_ptr T = L + OFF_RPB;
        const u32x4 b0 = lds_r128(T + 16 * iv[0]), b1 = lds_r128(T + 16 * iv[1]), b2 = lds_r128(T + 16 * iv[2]), b3 = lds_r128(T + 16 * iv[3]);
        lds_ptr D = L + OFF_BH + 1024 * f + 16 * lane;
#pragma unroll
        for (int hb = 0; hb < 4; ++hb) lds_w128(D + 9216 * hb, u32x4{b0[hb], b1[hb], b2[hb], b3[hb]});
    };
    asm volatile("s_waitcnt vmcnt(13)" : "+v"(ivA) :: "memory");
    bias_unit(wv, ivA);
    if (wv == 4) bias_unit(8, ivB);
    else if (wv == 5) {
        const f32x4 b48 = lds_rf128(L + OFF_RPB + 16 * ivB[0]);
        u32x4 pr;
#pragma unroll
        for (int hb = 0; hb < 4; ++hb) {
            const uint32_t hi = pk(b48[hb], 0.f) & 0xffffu;
            pr[hb] = hi | (pk(b48[hb] - bflo(hi), 0.f) << 16);
        }
        const int a48 = 128 * lane + 16 * (7 ^ ((lane >> 1) & 7));
        if (lane < 48) {
#pragma unroll
            for (int i = 0; i < 4; ++i) lds_w128(L + OFF_MASK + i * MS + a48, pr);
        }
    } else if (wv == 6) lds_w128(L + OFF_BL + 16 * lane, lds_r128(L + OFF_RPB + 16 * ivB[0]));
    WAIT_VM(0);
    BARRIER();
    PRIO_HOOK(0);

    u32x4 Wf[5][3];
    auto load_wf = [&](int s) {
        auto fr = [&](lds_ptr a) -> u32x4 {
            u32x2 lo = lds_tr(a + 18432 * s), hi = lds_tr(a + 18432 * s + 2304);
            return u32x4{lo[0], lo[1], hi[0], hi[1]};
        };
        Wf[0][s] = fr(wab);
        Wf[1][s] = fr(wab + 192);
        Wf[2][s] = fr(wc);
        Wf[3][s] = fr(wab + 384);
        const u32x4 t = fr(wv1);
#pragma unroll
        for (int r = 0; r < 4; ++r) Wf[4][s][r] = (uint32_t)__builtin_amdgcn_ds_bpermute(bperm, (int)t[r]) & bmask;
    };
    struct Acc { f32x4 a0, a1, a2, v0, v1; };
    auto proj_step = [&](Acc& A, u32x4 xf, int s, bool first) {
        A.a0 = mfma(Wf[0][s], xf, first ? ZERO : A.a0);
        A.a1 = mfma(Wf[1][s], xf, first ? ZERO : A.a1);
        A.a2 = mfma(Wf[2][s], xf, first ? ZERO : A.a2);
        A.v0 = mfma(xf, Wf[3][s], first ? ZERO : A.v0);
        A.v1 = mfma(xf, Wf[4][s], first ? ONES : A.v1);
    };
    auto proj_fin = [&](const Acc& A, u32x4& Q, u32x4& K, u32x2& V0, u32x2& V1) {
        const uint32_t c0 = pks(A.a2[0], A.a2[1], qsc), c1 = pks(A.a2[2], A.a2[3], qsc);
        const auto sw = __builtin_amdgcn_permlane16_swap(c0, c1, false, false);
        Q[0] = pks(A.a0[0], A.a0[1], QSCALE); Q[1] = pks(A.a0[2], A.a0[3], QSCALE); Q[2] = sw[0];
        K[0] = pk(A.a1[0], A.a1[1]); K[1] = pk(A.a1[2], A.a1[3]); K[2] = sw[1];
        V0 = u32x2{pk(A.v0[0], A.v0[1]), pk(A.v0[2], A.v0[3])};
        V1 = u32x2{pk(A.v1[0], A.v1[1]), pk(A.v1[2], A.v1[3])};
    };
    Frags F;
    F.Q[0][3] = F.Q[1][3] = F.Q[2][3] = 0u;
    F.K[0][3] = F.K[1][3] = F.K[2][3] = 0u;
    F.K[3][3] = (g == 0) ? 0x3FB90000u : (g == 1) ? 0xBB2C0000u : (g == 2) ? 0x3F803F80u : 0u;
    u32x2 V0L, V1L;
    f32x4 bias[3][3];
    float biasL;
    u32x2 V0[3], V1[3];
    auto set_vt = [&]() {
        F.VT[0][0] = u32x4{V0[0][0], V0[0][1], V0[1][0], V0[1][1]};
        F.VT[0][1] = u32x4{V0[2][0], V0[2][1], V0L[0], V0L[1]};
        F.VT[1][0] = u32x4{V1[0][0], V1[0][1], V1[1][0], V1[1][1]};
        F.VT[1][1] = u32x4{V1[2][0], V1[2][1], V1L[0], V1L[1]};
    };
    auto proj_tile = [&](lds_ptr a, u32x4& Q, u32x4& K, u32x2& v0, u32x2& v1) {
        u32x4 xf0 = lds_r128(a), xf1 = lds_r128(a + 64), xf2 = lds_r128(a + 128);
        Acc A;
        proj_step(A, xf0, 0, true); proj_step(A, xf1, 1, false); proj_step(A, xf2, 2, false);
        proj_fin(A, Q, K, v0, v1);
    };

    {
        lds_ptr xl = L + OFF_XL + 192 * (m >> 2) + 16 * g;
        const u32x4 xl0 = lds_r128(xl), xl1 = lds_r128(xl + 64), xl2 = lds_r128(xl + 128);
        load_wf(0); load_wf(1); load_wf(2);
        const u32x4 x00 = lds_r128(xa), x01 = lds_r128(xa + 64), x02 = lds_r128(xa + 128);
        {
            lds_ptr B = L + OFF_BH + 9216 * h + 16 * lane;
#pragma unroll
            for (int qt = 0; qt < 3; ++qt)
#pragma unroll
                for (int kt = 0; kt < 3; ++kt) bias[qt][kt] = lds_rf128(B + 1024 * (3 * qt + kt));
            biasL = lds_rf(L + OFF_BL + 16 * lane + 4 * h);
        }
        load_mask(p);
        {
            uint32_t v0 = xr[0], v1 = xr[1], v2 = xr[2];
            if (p == 1 && ww0 == 28) {
    #pragma unroll
                for (int j = 0; j < 3; ++j) {
                    const uint32_t pos = 144u * h + 64u * j + lane;
                    const uint32_t a = (pos * 781u) >> 16;
                    const uint32_t d = (pos - 84u * a >= 48u) ? (uint32_t)-43008 : 0u;
                    if (j == 0) v0 += d; else if (j == 1) v1 += d; else v2 += d;
                }
            }
            dma144nt(xm + 2688, v0, v1, v2, Lbase + OFF_X + (p + 2) * XS + 2304 * h);
        }
        load_mask(p + 2);
        Acc A, B;
        proj_step(A, xl0, 0, true); proj_step(A, xl1, 1, false); proj_step(A, xl2, 2, false);
        arrive(cnt_addr, 1u);
        proj_step(B, x00, 0, true); proj_step(B, x01, 1, false); proj_step(B, x02, 2, false);
        u32x4 QLr = {0u, 0u, 0u, 0u};
        proj_fin(A, QLr, F.K[3], V0L, V1L);
        if ((m & 3) == 0) lds_w128(QL + (m >> 2) * 64 + g * 16, QLr);
        proj_fin(B, F.Q[0], F.K[0], V0[0], V1[0]);
        proj_tile(xa + 3072, F.Q[1], F.K[1], V0[1], V1[1]);
        proj_tile(xa + 6144, F.Q[2], F.K[2], V0[2], V1[2]);
        set_vt();
    }
    WAIT_VM(2);
    arrive(cnt_addr, 16u << (4 * p));
    PRIO_HOOK(1);
    wait_fields(CNT, peek(CNT), 0xFu | (0xF0u << (4 * p)), 8u | (0x40u << (4 * p)));

    auto wo_frag = [&](int colb, int s) -> u32x4 {
        lds_ptr a = (s == 2 ? woa2 : woa0 + (s == 1 ? 384 : 0)) + colb;
        u32x2 lo = lds_tr(a), hi = lds_tr(a + 48 * 192);
        return u32x4{lo[0], lo[1], hi[0], hi[1]};
    };
    struct Pre { u32x4 ql, mk[3][3]; uint32_t q3[3], mL; };
    auto fetch = [&](auto itc, Pre& P) {
        constexpr int it = decltype(itc)::value;
        constexpr int MO = 2 * MS * it;
        P.ql = lds_r128(qa + 128 * it);
        P.mL = lds_r16(mLa + MO);
        uint32_t a0 = ma0;
        asm volatile("" : "+v"(a0));
        lds_ptr ma0p = (lds_ptr)(uintptr_t)a0, ma1p = (lds_ptr)(uintptr_t)(a0 ^ 32u), ma2p = (lds_ptr)(uintptr_t)(a0 ^ 64u);
#pragma unroll
        for (int tt = 0; tt < 3; ++tt) P.q3[tt] = lds_r32(q3a + MO + 2048 * tt);
#pragma unroll
        for (int tt = 0; tt < (it == 0 ? NPRE0 : 3); ++tt) {
            P.mk[tt][0] = lds_r128(ma0p + MO + 2048 * tt);
            P.mk[tt][1] = lds_r128(ma1p + MO + 2048 * tt);
            P.mk[tt][2] = lds_r128(ma2p + MO + 2048 * tt);
        }
    };
    auto window = [&](auto itc, Pre& P) {
        constexpr int it = decltype(itc)::value;
        constexpr int OO = 2 * OBS * it;
        const int i = p + 2 * it;
        uint32_t o[4][3];
        const f32x4 CW = {(g == i) ? 0.f : NEG_BIG, 0.f, 0.f, 0.f};
        const bool validL = (lane < 48) || (lane == 48 + 4 * i);
        uint32_t sb[4];
        asm volatile("" : "+v"(P.mL));
        const float cL = validL ? __builtin_fmaf(bflo(P.mL), LOG2E, biasL) : NEG_BIG;
        auto check = [&]() -> bool {
            const uint32_t lo = min(min(sb[0], sb[1]), min(sb[2], sb[3])), hi = max(max(sb[0], sb[1]), max(sb[2], sb[3]));
            const bool bad = (lo < 0x2B800000u) || (hi > 0x53800000u);
            return __builtin_amdgcn_ballot_w64(bad) != 0ull;
        };
        {
#pragma unroll
            for (int tt = 0; tt < 3; ++tt) F.Q[tt][3] = P.q3[tt];
            Sc S0, S1, S2;
            left_scores<false>(F, cL, validL, P.ql, PLw);
            const PLd pl = left_ld(PLr);
            scores(F, F.Q[0], bias[0], CW, P.mk[0], IHL, S0);
            if (it == 0) {
                uint32_t a0 = ma0;
                asm volatile("" : "+v"(a0));
                lds_ptr ma0p = (lds_ptr)(uintptr_t)a0, ma1p = (lds_ptr)(uintptr_t)(a0 ^ 32u), ma2p = (lds_ptr)(uintptr_t)(a0 ^ 64u);
#pragma unroll
                for (int tt = NPRE0; tt < 3; ++tt) {
                    P.mk[tt][0] = lds_r128(ma0p + 2048 * tt);
                    P.mk[tt][1] = lds_r128(ma1p + 2048 * tt);
                    P.mk[tt][2] = lds_r128(ma2p + 2048 * tt);
                }
            }
            SCHED_FENCE();
            scores(F, F.Q[1], bias[1], CW, P.mk[1], IHL, S1);
            exp_pv(F, S0, o[0], sb[0]);
            scores(F, F.Q[2], bias[2], CW, P.mk[2], IHL, S2);
            exp_pv(F, S1, o[1], sb[1]);
            left_pv(F, pl, o[3], sb[3]);
            exp_pv(F, S2, o[2], sb[2]);
        }
        if (__builtin_expect(check(), 0)) {
            lds_ptr M = L + OFF_MASK + i * MS;
#pragma unroll
            for (int qt = 0; qt < 3; ++qt) {
                Sc S;
                u32x4 Q = F.Q[qt];
                Q[3] = 0u;
                scores_safe(F, Q, bias[qt], CW, M + 2048 * qt, S);
                exp_pv(F, S, o[qt], sb[qt]);
            }
            left_scores<true>(F, cL, validL, lds_r128(qa + 128 * it), PLw);
            left_pv(F, left_ld(PLr), o[3], sb[3]);
        }
#pragma unroll
        for (int qt = 0; qt < 3; ++qt)
#pragma unroll
            for (int s = 0; s < 3; ++s) lds_w32(osw + OO + qt * 3072 + s * 1024, o[qt][s]);
        if (m == it) {
#pragma unroll
            for (int s = 0; s < 3; ++s) lds_w32(oLw + s * 1024, o[3][s]);
        }
        if (it == 0) WAIT_VM(0);
        arrive(cnt_addr, (it == 0 ? 0x1000u : 0x100000u) << (4 * p));
    };
    auto out_store = [&](const f32x4& c0, const f32x4& c1, uint32_t off) {
        u32x4 r = {pk(c0[0], c0[1]), pk(c0[2], c0[3]), pk(c1[0], c1[1]), pk(c1[2], c1[3])};
        gstore128(out, off, r);
    };

    Pre P;
    fetch(std::integral_constant<int, 0>{}, P);
    SCHED_FENCE();
    if (p == 0) {
        dma144((const char*)wo + 4608 * h, 16u * lane, Lbase + OFF_WO + 4608 * h);
        dma144((const char*)wo + 4608 * h + 2304, 16u * lane, Lbase + OFF_WO + 4608 * h + 2304);
    }
    window(std::integral_constant<int, 0>{}, P);
    PRIO_HOOK(2);
    proj_tile(xa + 2 * XS, F.Q[0], F.K[0], V0[0], V1[0]);
    proj_tile(xa + 2 * XS + 3072, F.Q[1], F.K[1], V0[1], V1[1]);
    proj_tile(xa + 2 * XS + 6144, F.Q[2], F.K[2], V0[2], V1[2]);
    set_vt();
    PRIO_HOOK(3);
    wait_fields(CNT, peek(CNT), p ? 0xFF000u : 0xF000u, p ? 0x44000u : 0x4000u);
    lds_ptr ord = (lds_ptr)(uintptr_t)((uint32_t)(uintptr_t)osw & ~15u);
    {
        u32x4 of[3][3], w[6];
        if (role < 3) {
#pragma unroll
            for (int s = 0; s < 3; ++s) {
                w[s] = wo_frag(0, s); w[3 + s] = wo_frag(8, s);
#pragma unroll
                for (int qt = 0; qt < 3; ++qt) of[qt][s] = lds_r128(ord + qt * 3072 + s * 1024);
            }
        }
        fetch(std::integral_constant<int, 1>{}, P);
        SCHED_FENCE();
        if (role < 3) {
            f32x4 c0[3], c1[3];
#pragma unroll
            for (int s = 0; s < 3; ++s)
#pragma unroll
                for (int qt = 0; qt < 3; ++qt) {
                    c0[qt] = mfma(w[s], of[qt][s], s ? c0[qt] : ZERO);
                    c1[qt] = mfma(w[3 + s], of[qt][s], s ? c1[qt] : ZERO);
                }
#pragma unroll
            for (int qt = 0; qt < 3; ++qt) out_store(c0[qt], c1[qt], so[qt]);
        }
    }
    PRIO_HOOK(4);
    window(std::integral_constant<int, 1>{}, P);
    PRIO_HOOK(5);
    if (role < 3) {
        uint32_t sx[3];
#pragma unroll
        for (int qt = 0; qt < 3; ++qt) sx[qt] = so[qt] + 2688u;
        if (ww0 + p + 2 == 31) {
            uint32_t mo = lane;
            asm volatile("" : "+v"(mo));
            mo &= 15u;
#pragma unroll
            for (int qt = 0; qt < 3; ++qt) {
                const uint32_t t = 16u * qt + mo, a = (t * 37u) >> 8;
                sx[qt] += (t - 7u * a >= 4u) ? (uint32_t)-43008 : 0u;
            }
        }
        u32x4 w[6];
#pragma unroll
        for (int s = 0; s < 3; ++s) { w[s] = wo_frag(0, s); w[3 + s] = wo_frag(8, s); }
        wait_fields(CNT, peek(CNT), 0xF00000u << (4 * p), 0x400000u << (4 * p));
        u32x4 of[3][3];
#pragma unroll
        for (int s = 0; s < 3; ++s)
#pragma unroll
            for (int qt = 0; qt < 3; ++qt) of[qt][s] = lds_r128(ord + 2 * OBS + qt * 3072 + s * 1024);
        f32x4 c0[3], c1[3];
#pragma unroll
        for (int s = 0; s < 3; ++s)
#pragma unroll
            for (int qt = 0; qt < 3; ++qt) {
                c0[qt] = mfma(w[s], of[qt][s], s ? c0[qt] : ZERO);
                c1[qt] = mfma(w[3 + s], of[qt][s], s ? c1[qt] : ZERO);
            }
#pragma unroll
        for (int qt = 0; qt < 3; ++qt) out_store(c0[qt], c1[qt], sx[qt]);
    } else {
        u32x4 w[3][6];
#pragma unroll
        for (int i2 = 0; i2 < 3; ++i2)
#pragma unroll
            for (int s = 0; s < 3; ++s) { w[i2][s] = wo_frag(64 * i2, s); w[i2][3 + s] = wo_frag(64 * i2 + 8, s); }
        int y = 7 * wh + 9; if (y >= 224) y -= 224;
        int xx = 7 * (ww0 + p + 2 * (m & 1)) + 9; if (xx >= 224) xx -= 224;
        const uint32_t dst = (uint32_t)((y * 224 + xx) * 192 + 16 * g);
        wait_fields(CNT, peek(CNT), 0xF00000u << (4 * p), 0x400000u << (4 * p));
        lds_ptr orr = L + OFF_OL + p * 3072 + lane * 16;
        u32x4 of0 = lds_r128(orr), of1 = lds_r128(orr + 1024), of2 = lds_r128(orr + 2048);
        f32x4 c0[3], c1[3];
#pragma unroll
        for (int i2 = 0; i2 < 3; ++i2) { c0[i2] = mfma(w[i2][0], of0, ZERO); c1[i2] = mfma(w[i2][3], of0, ZERO); }
#pragma unroll
        for (int i2 = 0; i2 < 3; ++i2) { c0[i2] = mfma(w[i2][1], of1, c0[i2]); c1[i2] = mfma(w[i2][4], of1, c1[i2]); }
#pragma unroll
        for (int i2 = 0; i2 < 3; ++i2) { c0[i2] = mfma(w[i2][2], of2, c0[i2]); c1[i2] = mfma(w[i2][5], of2, c1[i2]); }
        if (m < 2) {
#pragma unroll
            for (int i2 = 0; i2 < 3; ++i2) out_store(c0[i2], c1[i2], dst + 64 * i2);
        }
    }
}

extern "C" void kernel_launch(void* const* d_in, const int* in_sizes, int n_in, void* d_out, int out_size, void* d_ws,
                              size_t ws_size, hipStream_t stream) {
    (void)in_sizes; (void)n_in; (void)out_size; (void)d_ws; (void)ws_size;
    (void)hipFuncSetAttribute((const void*)wattn_kernel, hipFuncAttributeMaxDynamicSharedMemorySize, LDS_TOTAL);
    wattn_kernel<<<256, 512, LDS_TOTAL, stream>>>((const uint16_t*)d_in[0], (const uint16_t*)d_in[1], (const uint16_t*)d_in[2],
                                                  (const uint16_t*)d_in[3], (const int*)d_in[4], (const uint16_t*)d_in[5],
                                                  (uint16_t*)d_out);
}
```

```cpp
#include <hip/hip_runtime.h>
#include <hip/hip_bf16.h>
#include <stdint.h>
#include <type_traits>

typedef __attribute__((ext_vector_type(8))) __bf16 bf16x8;
typedef __attribute__((ext_vector_type(2))) __bf16 bf16x2;
typedef __attribute__((ext_vector_type(4))) float f32x4;
typedef __attribute__((ext_vector_type(2))) float f32x2;
typedef __attribute__((ext_vector_type(4))) uint32_t u32x4;
typedef __attribute__((ext_vector_type(2))) uint32_t u32x2;
typedef __attribute__((ext_vector_type(4))) short s16x4;

#define DEV __device__ __forceinline__
#define LAS __attribute__((address_space(3)))
typedef LAS char* lds_ptr;

constexpr int OFF_W = 0;
constexpr int OFF_OB = 0, OBS = 9216;
constexpr int OFF_OL = 36864;
constexpr int OFF_RPB = 55296;
constexpr int OFF_X = 58016, XS = 9216;
constexpr int OFF_XL = OFF_X + 4 * XS;
constexpr int OFF_MASK = (OFF_XL + 768 + 127) / 128 * 128, MS = 6272;
constexpr int OFF_WO = OFF_MASK + 4 * MS;
constexpr int OFF_BH = OFF_WO;
constexpr int OFF_QL = OFF_BH + 36864;
constexpr int OFF_PL = OFF_QL + 2048;
constexpr int OFF_BL = OFF_PL + 1024;
constexpr int OFF_CNT = OFF_BL + 1024;
constexpr int LDS_TOTAL = OFF_CNT + 64;
static_assert(LDS_TOTAL <= 163840 - 1024, "LDS");
static_assert(OFF_X % 16 == 0 && OFF_MASK % 128 == 0 && MS % 128 == 0 && OFF_WO % 16 == 0 && OFF_QL % 16 == 0, "align");

constexpr float LOG2E = 1.4426950408889634f;
constexpr float QSCALE = 0.20412414523193154f * 1.4426950408889634f;
constexpr float NEG_BIG = -1.0e30f;

DEV uint32_t pk(float a, float b) {
    f32x2 v = {a, b};
    bf16x2 r = __builtin_convertvector(v, bf16x2);
    return __builtin_bit_cast(uint32_t, r);
}
DEV uint32_t pks(float a, float b, float s) { return pk(a * s, __builtin_fmaf(b, s, 0.0f)); }
DEV f32x4 mfma(u32x4 a, u32x4 b, f32x4 c) {
    return __builtin_amdgcn_mfma_f32_16x16x32_bf16(__builtin_bit_cast(bf16x8, a), __builtin_bit_cast(bf16x8, b), c, 0, 0, 0);
}
DEV u32x4 lds_r128(lds_ptr p) { return *(const LAS u32x4*)p; }
DEV f32x4 lds_rf128(lds_ptr p) { return *(const LAS f32x4*)p; }
DEV u32x2 lds_r64(lds_ptr p) { return *(const LAS u32x2*)p; }
DEV uint32_t lds_r32(lds_ptr p) { return *(const LAS uint32_t*)p; }
DEV float lds_rf(lds_ptr p) { return *(const LAS float*)p; }
DEV uint32_t lds_r16(lds_ptr p) { return (uint32_t) * (const LAS uint16_t*)p; }
DEV void lds_w128(lds_ptr p, u32x4 v) { *(LAS u32x4*)p = v; }
DEV void lds_w32(lds_ptr p, uint32_t v) { *(LAS uint32_t*)p = v; }
DEV void lds_w16(lds_ptr p, uint32_t v) { *(LAS uint16_t*)p = (uint16_t)v; }
DEV u32x2 lds_tr(lds_ptr p) {
    return __builtin_bit_cast(u32x2, __builtin_amdgcn_ds_read_tr16_b64_v4i16((LAS s16x4*)p));
}
DEV float bflo(uint32_t v) { return __builtin_bit_cast(float, v << 16); }
DEV float bfhi(uint32_t v) { return __builtin_bit_cast(float, v & 0xffff0000u); }
DEV float ex2(float v) { return __builtin_amdgcn_exp2f(v); }

DEV void dma16nt(const void* sbase, uint32_t voff, uint32_t lds_dst) {
    unsigned keep;
    asm volatile("s_mov_b32 %0, m0\n\ts_mov_b32 m0, %3\n\ts_nop 0\n\tglobal_load_lds_dwordx4 %1, %2 nt\n\ts_mov_b32 m0, %0"
                 : "=&s"(keep) : "v"(voff), "s"(sbase), "s"(lds_dst) : "memory");
}
DEV void dma144(const void* sbase, uint32_t voff, uint32_t lds_dst) {
    unsigned keep;
    asm volatile("s_mov_b32 %0, m0\n\ts_mov_b32 m0, %3\n\ts_nop 0\n\t"
                 "global_load_lds_dwordx4 %1, %2\n\t"
                 "global_load_lds_dwordx4 %1, %2 offset:1024\n\t"
                 "s_mov_b64 exec, 0xffff\n\t"
                 "global_load_lds_dwordx4 %1, %2 offset:2048\n\t"
                 "s_mov_b64 exec, -1\n\ts_mov_b32 m0, %0"
                 : "=&s"(keep) : "v"(voff), "s"(sbase), "s"(lds_dst) : "memory");
}
DEV void dma144nt(const void* sbase, uint32_t v0, uint32_t v1, uint32_t v2, uint32_t lds_dst) {
    unsigned keep;
    asm volatile("s_mov_b32 %0, m0\n\ts_mov_b32 m0, %5\n\ts_nop 0\n\t"
                 "global_load_lds_dwordx4 %1, %4 nt\n\t"
                 "global_load_lds_dwordx4 %2, %4 offset:1024 nt\n\t"
                 "s_mov_b64 exec, 0xffff\n\t"
                 "global_load_lds_dwordx4 %3, %4 offset:2048 nt\n\t"
                 "s_mov_b64 exec, -1\n\ts_mov_b32 m0, %0"
                 : "=&s"(keep) : "v"(v0), "v"(v1), "v"(v2), "s"(sbase), "s"(lds_dst) : "memory");
}
DEV u32x4 gload128(const void* sbase, uint32_t voff) {
    u32x4 v;
    asm volatile("global_load_dwordx4 %0, %1, %2" : "=v"(v) : "v"(voff), "s"(sbase) : "memory");
    return v;
}
DEV uint32_t gload32(const void* sbase, uint32_t voff) {
    uint32_t v;
    asm volatile("global_load_dword %0, %1, %2" : "=v"(v) : "v"(voff), "s"(sbase) : "memory");
    return v;
}
DEV void gstore128(void* sbase, uint32_t voff, u32x4 v) {
    asm volatile("global_store_dwordx4 %0, %1, %2 sc0 sc1\n\ts_nop 1" :: "v"(voff), "v"(v), "s"(sbase) : "memory");
}
#define BARRIER() do { asm volatile("s_waitcnt lgkmcnt(0)" ::: "memory"); __builtin_amdgcn_s_barrier(); asm volatile("" ::: "memory"); } while (0)
#define WAIT_VM(n) asm volatile("s_waitcnt vmcnt(" #n ")" ::: "memory")

DEV void arrive(uint32_t cnt_addr, uint32_t inc) {
    uint32_t t0, t1;
    asm volatile("v_mov_b32 %0, %2\n\tv_mov_b32 %1, %3\n\ts_mov_b64 exec, 1\n\t"
                 "ds_add_u32 %0, %1\n\ts_mov_b64 exec, -1"
                 : "=&v"(t0), "=&v"(t1) : "s"(cnt_addr), "s"(inc) : "memory");
}
DEV uint32_t peek(lds_ptr cnt) { return __hip_atomic_load((LAS uint32_t*)cnt, __ATOMIC_RELAXED, __HIP_MEMORY_SCOPE_WORKGROUP); }
DEV void wait_fields(lds_ptr cnt, uint32_t v, uint32_t mask, uint32_t target) {
    while (((uint32_t)__builtin_amdgcn_readfirstlane(v) & mask) != target) v = peek(cnt);
    asm volatile("" ::: "memory");
}

struct Frags {
    u32x4 Q[3], K[4];
    u32x4 VT[2][2];
};

DEV void pv_norm(const Frags& F, u32x4 P0, u32x4 P1, uint32_t (&o)[3], uint32_t& sumbits) {
    f32x4 zero = {0.f, 0.f, 0.f, 0.f};
    f32x4 oa = mfma(F.VT[0][0], P0, zero);
    f32x4 ob = mfma(F.VT[1][0], P0, zero);
    oa = mfma(F.VT[0][1], P1, oa);
    ob = mfma(F.VT[1][1], P1, ob);
    float sum = ob[2];
    sumbits = __builtin_bit_cast(uint32_t, sum);
    float rs = __builtin_amdgcn_rcpf(sum);
    o[0] = pks(oa[0], oa[1], rs);
    o[1] = pks(oa[2], oa[3], rs);
    o[2] = pks(ob[0], ob[1], rs);
}

struct Sc { f32x4 s0, s1, s2; float s30; };
DEV void scores(const Frags& F, u32x4 Q, const f32x4 (&bias)[3], f32x4 CW, const u32x4 (&mk)[3], u32x4 IHL, Sc& S) {
    S.s0 = mfma(F.K[0], Q, bias[0]);
    S.s1 = mfma(F.K[1], Q, bias[1]);
    S.s2 = mfma(F.K[2], Q, bias[2]);
    f32x4 s3 = mfma(F.K[3], Q, CW);
    S.s30 = s3[0];
    S.s0 = mfma(IHL, mk[0], S.s0);
    S.s1 = mfma(IHL, mk[1], S.s1);
    S.s2 = mfma(IHL, mk[2], S.s2);
}
DEV void exp_pv(const Frags& F, const Sc& S, uint32_t (&o)[3], uint32_t& sumbits) {
    u32x4 P0 = {pk(ex2(S.s0[0]), ex2(S.s0[1])), pk(ex2(S.s0[2]), ex2(S.s0[3])), pk(ex2(S.s1[0]), ex2(S.s1[1])), pk(ex2(S.s1[2]), ex2(S.s1[3]))};
    u32x4 P1 = {pk(ex2(S.s2[0]), ex2(S.s2[1])), pk(ex2(S.s2[2]), ex2(S.s2[3])), pk(ex2(S.s30), 0.f), 0u};
    pv_norm(F, P0, P1, o, sumbits);
}
DEV void scores_safe(const Frags& F, u32x4 Q, const f32x4 (&bias)[3], f32x4 CW, lds_ptr M, Sc& S) {
    const int lane = threadIdx.x & 63;
    const int g = lane >> 4, mm = lane & 15;
    const int v = (mm >> 1) & 7;
    f32x4 zero = {0.f, 0.f, 0.f, 0.f};
    f32x4 s0 = mfma(F.K[0], Q, bias[0]);
    f32x4 s1 = mfma(F.K[1], Q, bias[1]);
    f32x4 s2 = mfma(F.K[2], Q, bias[2]);
    f32x4 s3 = mfma(F.K[3], Q, zero);
    const int h4 = 4 * (__builtin_amdgcn_readfirstlane(threadIdx.x >> 6) & 3);
    const uint32_t bb = lds_r32(M + 128 * mm + 16 * (7 ^ v) + h4);
    const float m48 = bflo(lds_r16(M + 128 * mm + 16 * (6 ^ v) + 14));
    float s30 = __builtin_fmaf(m48, LOG2E, s3[0] + (bflo(bb) + bfhi(bb)));
    u32x2 mv0 = lds_r64(M + 128 * mm + 16 * ((0 + (g >> 1)) ^ v) + 8 * (g & 1));
    u32x2 mv1 = lds_r64(M + 128 * mm + 16 * ((2 + (g >> 1)) ^ v) + 8 * (g & 1));
    u32x2 mv2 = lds_r64(M + 128 * mm + 16 * ((4 + (g >> 1)) ^ v) + 8 * (g & 1));
#define ADDM(s, mv) s[0] = __builtin_fmaf(bflo(mv[0]), LOG2E, s[0]); s[1] = __builtin_fmaf(bfhi(mv[0]), LOG2E, s[1]); \
                    s[2] = __builtin_fmaf(bflo(mv[1]), LOG2E, s[2]); s[3] = __builtin_fmaf(bfhi(mv[1]), LOG2E, s[3]);
    ADDM(s0, mv0) ADDM(s1, mv1) ADDM(s2, mv2)
#undef ADDM
    s30 = (CW[0] == 0.f) ? s30 : -INFINITY;
    float mx = s30;
#pragma unroll
    for (int r = 0; r < 4; ++r) mx = fmaxf(mx, fmaxf(s0[r], fmaxf(s1[r], s2[r])));
    mx = fmaxf(mx, __shfl_xor(mx, 16));
    mx = fmaxf(mx, __shfl_xor(mx, 32));
#pragma unroll
    for (int r = 0; r < 4; ++r) { s0[r] -= mx; s1[r] -= mx; s2[r] -= mx; }
    S.s0 = s0; S.s1 = s1; S.s2 = s2; S.s30 = s30 - mx;
}

template <bool SAFE>
DEV void left_scores(const Frags& F, float cinit, bool valid, u32x4 ql, lds_ptr PLw) {
    const int g = (threadIdx.x & 63) >> 4;
    const f32x4 c = {cinit, 0.f, 0.f, 0.f};
    const f32x4 s0 = mfma(ql, F.K[0], c), s1 = mfma(ql, F.K[1], c), s2 = mfma(ql, F.K[2], c), s3 = mfma(ql, F.K[3], c);
    const float va = (g & 1) ? s1[0] : s0[0], vb = (g & 1) ? s3[0] : s2[0];
    float v = (g & 2) ? vb : va;
    if (SAFE) {
        v = valid ? v : -INFINITY;
        float mx = v;
#pragma unroll
        for (int d = 1; d < 64; d <<= 1) mx = fmaxf(mx, __shfl_xor(mx, d));
        v -= mx;
    }
    lds_w16(PLw, pk(ex2(v), 0.f));
}
struct PLd { u32x2 a0, a1, a2, a3; };
DEV PLd left_ld(lds_ptr PLr) { return PLd{lds_r64(PLr), lds_r64(PLr + 32), lds_r64(PLr + 64), lds_r64(PLr + 96)}; }
DEV void left_pv(const Frags& F, const PLd& a, uint32_t (&o)[3], uint32_t& sumbits) {
    u32x4 P0 = {a.a0[0], a.a0[1], a.a1[0], a.a1[1]}, P1 = {a.a2[0], a.a2[1], a.a3[0], a.a3[1]};
    pv_norm(F, P0, P1, o, sumbits);
}
#define PR0 -1
#define PR1 1
#define PR0B -1
#define PR1B 5
#define PRIO_HOOK(k) do { if (PR1 == k) { if (p == 1) __builtin_amdgcn_s_setprio(1); } if (PR1B == k) { if (p == 1) __builtin_amdgcn_s_setprio(0); } \
    if (PR0 == k) { if (p == 0) __builtin_amdgcn_s_setprio(2); } if (PR0B == k) { if (p == 0) __builtin_amdgcn_s_setprio(0); } } while (0)
#define SCHED_FENCE() __builtin_amdgcn_sched_barrier(0)
#define NPRE0 2

__global__ __launch_bounds__(512, 2) void wattn_kernel(const uint16_t* __restrict__ x, const uint16_t* __restrict__ wqkv,
                                                       const uint16_t* __restrict__ wo, const uint16_t* __restrict__ rpb,
                                                       const int* __restrict__ rpidx, const uint16_t* __restrict__ mask,
                                                       uint16_t* __restrict__ out) {
    extern __shared__ __attribute__((aligned(16))) char smem_[];
    const unsigned long long tz0_ = __builtin_amdgcn_s_memrealtime();
    unsigned long long tz1_ = tz0_;
    lds_ptr L = (lds_ptr)smem_;
    const uint32_t Lbase = (uint32_t)(uintptr_t)L;
    const int tid = threadIdx.x, lane = tid & 63;
    const int wv = __builtin_amdgcn_readfirstlane(tid >> 6);
    const int h = wv & 3, p = wv >> 2;
    const int g = lane >> 4, m = lane & 15;
    const int blk = blockIdx.x;
    const int wh = blk >> 3, ww0 = (blk & 7) * 4;
    const uint32_t wrapY = (wh == 31) ? (uint32_t)-9633792 : 0u;
    const uint32_t org0 = (uint32_t)(((7 * wh + 3) * 224 + 7 * ww0 + 3) * 192);
    const int role = (h + 2 * p) & 3;

    const uint32_t rsrc = 4 * (tid < 338 ? tid : 337);
    const uint32_t l48 = lane < 48 ? lane : 48;
    const uint32_t ilo = 196u * m + 16u * g;
    const int fq = (wv * 11) >> 5;
    const uint32_t isrcA = ilo + (uint32_t)(3136 * fq + 64 * (wv - 3 * fq));
    const uint32_t isrcB = (wv == 4) ? ilo + 6400u : (wv == 5) ? 196u * l48 + 192u : 9408u + 4u * l48;
    uint32_t xr[3];
#pragma unroll
    for (int j = 0; j < 3; ++j) {
        const uint32_t pos = 144u * h + 64u * j + lane;
        const uint32_t a = (pos * 781u) >> 16, p96 = (pos * 683u) >> 16;
        xr[j] = ((pos << 4) ^ ((p96 & 1u) << 5)) + 41664u * a + (pos >= 336u ? wrapY : 0u) + (org0 + 1344u * p + 8192u - 1024u * j);
    }
    const uint32_t wvoff = (16u * lane) ^ (uint32_t)(32 * ((wv >> 1) & 1));
    uint32_t msrc[2];
    bool mok[2];
#pragma unroll
    for (int j = 0; j < 2; ++j) {
        const int pos = 98 * h + 64 * j + lane;
        const int q = pos >> 3, mk = (pos & 7) ^ ((q >> 1) & 7);
        msrc[j] = 98 * q + (mk < 6 ? 16 * mk : 82);
        mok[j] = (mk < 7) && (j == 0 || lane < 34);
    }
    if (wv == 7 && lane == 0) lds_w32(L + OFF_CNT, 0u);
    asm volatile("" :: "v"(rsrc), "v"(isrcA), "v"(isrcB), "v"(xr[0]), "v"(xr[1]), "v"(xr[2]), "v"(wvoff), "v"(msrc[0]), "v"(msrc[1]));
    const float onec = ((m & 3) >= 2) ? 1.0f : 0.0f;
    const f32x4 ONES = {onec, onec, onec, onec};
    const f32x4 ZERO = {0.f, 0.f, 0.f, 0.f};
    const float qsc = (g & 1) ? 1.0f : QSCALE;
    lds_ptr QL = L + OFF_QL + wv * 256;
    lds_ptr CNT = L + OFF_CNT;
    const uint32_t cnt_addr = Lbase + OFF_CNT;
    lds_ptr wab, wc, wv1;
    {
        const int q4 = m >> 2, p4 = m & 3, sx = 32 * (g & 1);
        lds_ptr rb = L + OFF_W + 576 * (8 * g + q4);
        wab = rb + ((48 * h + 8 * p4) ^ sx);
        wc = rb + ((48 * h + 32 + 8 * (p4 >> 1)) ^ sx) + 192 * (p4 & 1);
        wv1 = rb + 384 + ((48 * h + 32 + 8 * (p4 & 1)) ^ sx);
    }
    const int bperm = 4 * (16 * g + 2 * (m >> 2) + (m & 1));
    const uint32_t bmask = ((m & 3) < 2) ? 0xffffffffu : 0u;
    u32x4 IHL;
    {
        const uint32_t val = (g < 2) ? 0x3FB9u : 0xBB2Cu;
        const int j = m - 8 * (g & 1);
#pragma unroll
        for (int r = 0; r < 4; ++r) IHL[r] = (j == 2 * r) ? val : (j == 2 * r + 1) ? (val << 16) : 0u;
    }
    lds_ptr q3a, mLa, qa, osw, oLw, xa, woa0, woa2, PLw, PLr;
    uint32_t ma0;
    {
        const int vsw = (m >> 1) & 7;
        lds_ptr M = L + OFF_MASK + p * MS;
        ma0 = Lbase + OFF_MASK + p * MS + 128 * m + 16 * ((g & 1) ^ vsw);
        q3a = M + 128 * m + (g < 2 ? 16 * (6 ^ vsw) + 12 : 16 * (7 ^ vsw) + 4 * h);
        mLa = M + 128 * 48 + (lane < 48 ? 2 * lane : 110);
        qa = QL + 64 * p + 16 * g;
        osw = L + OFF_OB + p * OBS + lane * 16 + 4 * (h ^ (g & 1));
        oLw = L + OFF_OL + p * 3072 + lane * 16 + 4 * (h ^ (g & 1));
        xa = L + OFF_X + p * XS + 192 * m + ((16 * g) ^ (((m >> 3) & 1) << 5));
        const int q4 = m >> 2, p4 = m & 3;
        const int rb = 24 * ((q4 >> 1) ^ (g & 1)) + (q4 & 1);
        const int cb = (role < 3) ? 64 * role : 0;
        woa0 = L + OFF_WO + (rb + 4 * g) * 192 + 16 * p4 + cb;
        woa2 = L + OFF_WO + (rb + 16 + 2 * g) * 192 + 16 * p4 + cb;
        PLw = L + OFF_PL + wv * 128 + 2 * lane;
        PLr = L + OFF_PL + wv * 128 + 8 * g;
    }
    uint32_t so[3];
#pragma unroll
    for (int qt = 0; qt < 3; ++qt) {
        const uint32_t t = 16u * qt + m, a = (t * 37u) >> 8;
        so[qt] = 192u * t + 41664u * a + (a >= 4u ? wrapY : 0u) + 16u * g + (uint32_t)(64 * role) + (org0 + 1344u * p);
    }
    asm volatile("" : "+v"(so[0]), "+v"(so[1]), "+v"(so[2]), "+v"(IHL), "+v"(ma0), "+v"(q3a), "+v"(mLa));
    asm volatile("" : "+v"(qa), "+v"(osw), "+v"(xa), "+v"(woa0), "+v"(woa2));
    asm volatile("" : "+v"(wab), "+v"(wc), "+v"(wv1));

    const char* xm = (const char*)x - 8192;
    auto load_w = [&](int s) {
        dma144((const char*)wqkv + 18432 * s + 2304 * wv, wvoff, Lbase + OFF_W + 18432 * s + 2304 * wv);
    };
    auto load_mask = [&](int i) {
        const char* mb = (const char*)mask + (size_t)(4 * blk + i) * 4802;
        const uint32_t dst = Lbase + OFF_MASK + i * MS + 98 * 16 * h;
        if (mok[0]) dma16nt(mb, msrc[0], dst);
        if (mok[1]) dma16nt(mb, msrc[1], dst + 1024);
    };
    u32x4 ivA, ivB = {0u, 0u, 0u, 0u};
    if (wv == 4) ivB = gload128(rpidx, isrcB);
    else if (wv == 5 || wv == 6) ivB[0] = gload32(rpidx, isrcB);
    uint32_t rpbv = gload32(rpb, rsrc);
    ivA = gload128(rpidx, isrcA);
    {
        int y = 7 * wh + 9; if (y >= 224) y -= 224;
        int xx = 7 * (ww0 + (wv >> 1)) + 9; if (xx >= 224) xx -= 224;
        if (lane < 6) dma16nt(x, (uint32_t)((y * 224 + xx) * 192 + 96 * (wv & 1) + 16 * lane), Lbase + OFF_XL + 96 * wv);
    }
    dma144nt(xm, xr[0], xr[1], xr[2], Lbase + OFF_X + p * XS + 2304 * h);
    load_w(0);
    load_w(1);

    asm volatile("s_waitcnt vmcnt(11)" : "+v"(rpbv), "+v"(ivB) :: "memory");
    if (tid < 338) *(LAS f32x2*)(L + OFF_RPB + 8 * tid) = f32x2{bflo(rpbv) * LOG2E, bfhi(rpbv) * LOG2E};
    BARRIER();
    load_w(2);
    auto bias_unit = [&](int f, u32x4 iv) {
        lds_ptr T = L + OFF_RPB;
        const u32x4 b0 = lds_r128(T + 16 * iv[0]), b1 = lds_r128(T + 16 * iv[1]), b2 = lds_r128(T + 16 * iv[2]), b3 = lds_r128(T + 16 * iv[3]);
        lds_ptr D = L + OFF_BH + 1024 * f + 16 * lane;
#pragma unroll
        for (int hb = 0; hb < 4; ++hb) lds_w128(D + 9216 * hb, u32x4{b0[hb], b1[hb], b2[hb], b3[hb]});
    };
    asm volatile("s_waitcnt vmcnt(13)" : "+v"(ivA) :: "memory");
    bias_unit(wv, ivA);
    if (wv == 4) bias_unit(8, ivB);
    else if (wv == 5) {
        const f32x4 b48 = lds_rf128(L + OFF_RPB + 16 * ivB[0]);
        u32x4 pr;
#pragma unroll
        for (int hb = 0; hb < 4; ++hb) {
            const uint32_t hi = pk(b48[hb], 0.f) & 0xffffu;
            pr[hb] = hi | (pk(b48[hb] - bflo(hi), 0.f) << 16);
        }
        const int a48 = 128 * lane + 16 * (7 ^ ((lane >> 1) & 7));
        if (lane < 48) {
#pragma unroll
            for (int i = 0; i < 4; ++i) lds_w128(L + OFF_MASK + i * MS + a48, pr);
        }
    } else if (wv == 6) lds_w128(L + OFF_BL + 16 * lane, lds_r128(L + OFF_RPB + 16 * ivB[0]));
    WAIT_VM(0);
    BARRIER();
    PRIO_HOOK(0);

    u32x4 Wf[5][3];
    auto load_wf = [&](int s) {
        auto fr = [&](lds_ptr a) -> u32x4 {
            u32x2 lo = lds_tr(a + 18432 * s), hi = lds_tr(a + 18432 * s + 2304);
            return u32x4{lo[0], lo[1], hi[0], hi[1]};
        };
        Wf[0][s] = fr(wab);
        Wf[1][s] = fr(wab + 192);
        Wf[2][s] = fr(wc);
        Wf[3][s] = fr(wab + 384);
        const u32x4 t = fr(wv1);
#pragma unroll
        for (int r = 0; r < 4; ++r) Wf[4][s][r] = (uint32_t)__builtin_amdgcn_ds_bpermute(bperm, (int)t[r]) & bmask;
    };
    struct Acc { f32x4 a0, a1, a2, v0, v1; };
    auto proj_step = [&](Acc& A, u32x4 xf, int s, bool first) {
        A.a0 = mfma(Wf[0][s], xf, first ? ZERO : A.a0);
        A.a1 = mfma(Wf[1][s], xf, first ? ZERO : A.a1);
        A.a2 = mfma(Wf[2][s], xf, first ? ZERO : A.a2);
        A.v0 = mfma(xf, Wf[3][s], first ? ZERO : A.v0);
        A.v1 = mfma(xf, Wf[4][s], first ? ONES : A.v1);
    };
    auto proj_fin = [&](const Acc& A, u32x4& Q, u32x4& K, u32x2& V0, u32x2& V1) {
        const uint32_t c0 = pks(A.a2[0], A.a2[1], qsc), c1 = pks(A.a2[2], A.a2[3], qsc);
        const auto sw = __builtin_amdgcn_permlane16_swap(c0, c1, false, false);
        Q[0] = pks(A.a0[0], A.a0[1], QSCALE); Q[1] = pks(A.a0[2], A.a0[3], QSCALE); Q[2] = sw[0];
        K[0] = pk(A.a1[0], A.a1[1]); K[1] = pk(A.a1[2], A.a1[3]); K[2] = sw[1];
        V0 = u32x2{pk(A.v0[0], A.v0[1]), pk(A.v0[2], A.v0[3])};
        V1 = u32x2{pk(A.v1[0], A.v1[1]), pk(A.v1[2], A.v1[3])};
    };
    Frags F;
    F.Q[0][3] = F.Q[1][3] = F.Q[2][3] = 0u;
    F.K[0][3] = F.K[1][3] = F.K[2][3] = 0u;
    F.K[3][3] = (g == 0) ? 0x3FB90000u : (g == 1) ? 0xBB2C0000u : (g == 2) ? 0x3F803F80u : 0u;
    u32x2 V0L, V1L;
    f32x4 bias[3][3];
    float biasL;
    u32x2 V0[3], V1[3];
    auto set_vt = [&]() {
        F.VT[0][0] = u32x4{V0[0][0], V0[0][1], V0[1][0], V0[1][1]};
        F.VT[0][1] = u32x4{V0[2][0], V0[2][1], V0L[0], V0L[1]};
        F.VT[1][0] = u32x4{V1[0][0], V1[0][1], V1[1][0], V1[1][1]};
        F.VT[1][1] = u32x4{V1[2][0], V1[2][1], V1L[0], V1L[1]};
    };
    auto proj_tile = [&](lds_ptr a, u32x4& Q, u32x4& K, u32x2& v0, u32x2& v1) {
        u32x4 xf0 = lds_r128(a), xf1 = lds_r128(a + 64), xf2 = lds_r128(a + 128);
        Acc A;
        proj_step(A, xf0, 0, true); proj_step(A, xf1, 1, false); proj_step(A, xf2, 2, false);
        proj_fin(A, Q, K, v0, v1);
    };

    {
        lds_ptr xl = L + OFF_XL + 192 * (m >> 2) + 16 * g;
        const u32x4 xl0 = lds_r128(xl), xl1 = lds_r128(xl + 64), xl2 = lds_r128(xl + 128);
        load_wf(0); load_wf(1); load_wf(2);
        const u32x4 x00 = lds_r128(xa), x01 = lds_r128(xa + 64), x02 = lds_r128(xa + 128);
        {
            lds_ptr B = L + OFF_BH + 9216 * h + 16 * lane;
#pragma unroll
            for (int qt = 0; qt < 3; ++qt)
#pragma unroll
                for (int kt = 0; kt < 3; ++kt) bias[qt][kt] = lds_rf128(B + 1024 * (3 * qt + kt));
            biasL = lds_rf(L + OFF_BL + 16 * lane + 4 * h);
        }
        load_mask(p);
        {
            uint32_t v0 = xr[0], v1 = xr[1], v2 = xr[2];
            if (p == 1 && ww0 == 28) {
    #pragma unroll
                for (int j = 0; j < 3; ++j) {
                    const uint32_t pos = 144u * h + 64u * j + lane;
                    const uint32_t a = (pos * 781u) >> 16;
                    const uint32_t d = (pos - 84u * a >= 48u) ? (uint32_t)-43008 : 0u;
                    if (j == 0) v0 += d; else if (j == 1) v1 += d; else v2 += d;
                }
            }
            dma144nt(xm + 2688, v0, v1, v2, Lbase + OFF_X + (p + 2) * XS + 2304 * h);
        }
        load_mask(p + 2);
        Acc A, B;
        proj_step(A, xl0, 0, true); proj_step(A, xl1, 1, false); proj_step(A, xl2, 2, false);
        arrive(cnt_addr, 1u);
        proj_step(B, x00, 0, true); proj_step(B, x01, 1, false); proj_step(B, x02, 2, false);
        u32x4 QLr = {0u, 0u, 0u, 0u};
        proj_fin(A, QLr, F.K[3], V0L, V1L);
        if ((m & 3) == 0) lds_w128(QL + (m >> 2) * 64 + g * 16, QLr);
        proj_fin(B, F.Q[0], F.K[0], V0[0], V1[0]);
        proj_tile(xa + 3072, F.Q[1], F.K[1], V0[1], V1[1]);
        proj_tile(xa + 6144, F.Q[2], F.K[2], V0[2], V1[2]);
        set_vt();
    }
    WAIT_VM(2);
    arrive(cnt_addr, 16u << (4 * p));
    PRIO_HOOK(1);
    wait_fields(CNT, peek(CNT), 0xFu | (0xF0u << (4 * p)), 8u | (0x40u << (4 * p)));

    auto wo_frag = [&](int colb, int s) -> u32x4 {
        lds_ptr a = (s == 2 ? woa2 : woa0 + (s == 1 ? 384 : 0)) + colb;
        u32x2 lo = lds_tr(a), hi = lds_tr(a + 48 * 192);
        return u32x4{lo[0], lo[1], hi[0], hi[1]};
    };
    struct Pre { u32x4 ql, mk[3][3]; uint32_t q3[3], mL; };
    auto fetch = [&](auto itc, Pre& P) {
        constexpr int it = decltype(itc)::value;
        constexpr int MO = 2 * MS * it;
        P.ql = lds_r128(qa + 128 * it);
        P.mL = lds_r16(mLa + MO);
        uint32_t a0 = ma0;
        asm volatile("" : "+v"(a0));
        lds_ptr ma0p = (lds_ptr)(uintptr_t)a0, ma1p = (lds_ptr)(uintptr_t)(a0 ^ 32u), ma2p = (lds_ptr)(uintptr_t)(a0 ^ 64u);
#pragma unroll
        for (int tt = 0; tt < 3; ++tt) P.q3[tt] = lds_r32(q3a + MO + 2048 * tt);
#pragma unroll
        for (int tt = 0; tt < (it == 0 ? NPRE0 : 3); ++tt) {
            P.mk[tt][0] = lds_r128(ma0p + MO + 2048 * tt);
            P.mk[tt][1] = lds_r128(ma1p + MO + 2048 * tt);
            P.mk[tt][2] = lds_r128(ma2p + MO + 2048 * tt);
        }
    };
    auto window = [&](auto itc, Pre& P) {
        constexpr int it = decltype(itc)::value;
        constexpr int OO = 2 * OBS * it;
        const int i = p + 2 * it;
        uint32_t o[4][3];
        const f32x4 CW = {(g == i) ? 0.f : NEG_BIG, 0.f, 0.f, 0.f};
        const bool validL = (lane < 48) || (lane == 48 + 4 * i);
        uint32_t sb[4];
        asm volatile("" : "+v"(P.mL));
        const float cL = validL ? __builtin_fmaf(bflo(P.mL), LOG2E, biasL) : NEG_BIG;
        auto check = [&]() -> bool {
            const uint32_t lo = min(min(sb[0], sb[1]), min(sb[2], sb[3])), hi = max(max(sb[0], sb[1]), max(sb[2], sb[3]));
            const bool bad = (lo < 0x2B800000u) || (hi > 0x53800000u);
            return __builtin_amdgcn_ballot_w64(bad) != 0ull;
        };
        {
#pragma unroll
            for (int tt = 0; tt < 3; ++tt) F.Q[tt][3] = P.q3[tt];
            Sc S0, S1, S2;
            left_scores<false>(F, cL, validL, P.ql, PLw);
            const PLd pl = left_ld(PLr);
            scores(F, F.Q[0], bias[0], CW, P.mk[0], IHL, S0);
            if (it == 0) {
                uint32_t a0 = ma0;
                asm volatile("" : "+v"(a0));
                lds_ptr ma0p = (lds_ptr)(uintptr_t)a0, ma1p = (lds_ptr)(uintptr_t)(a0 ^ 32u), ma2p = (lds_ptr)(uintptr_t)(a0 ^ 64u);
#pragma unroll
                for (int tt = NPRE0; tt < 3; ++tt) {
                    P.mk[tt][0] = lds_r128(ma0p + 2048 * tt);
                    P.mk[tt][1] = lds_r128(ma1p + 2048 * tt);
                    P.mk[tt][2] = lds_r128(ma2p + 2048 * tt);
                }
            }
            SCHED_FENCE();
            scores(F, F.Q[1], bias[1], CW, P.mk[1], IHL, S1);
            exp_pv(F, S0, o[0], sb[0]);
            scores(F, F.Q[2], bias[2], CW, P.mk[2], IHL, S2);
            exp_pv(F, S1, o[1], sb[1]);
            left_pv(F, pl, o[3], sb[3]);
            exp_pv(F, S2, o[2], sb[2]);
        }
        if (__builtin_expect(check(), 0)) {
            lds_ptr M = L + OFF_MASK + i * MS;
#pragma unroll
            for (int qt = 0; qt < 3; ++qt) {
                Sc S;
                u32x4 Q = F.Q[qt];
                Q[3] = 0u;
                scores_safe(F, Q, bias[qt], CW, M + 2048 * qt, S);
                exp_pv(F, S, o[qt], sb[qt]);
            }
            left_scores<true>(F, cL, validL, lds_r128(qa + 128 * it), PLw);
            left_pv(F, left_ld(PLr), o[3], sb[3]);
        }
#pragma unroll
        for (int qt = 0; qt < 3; ++qt)
#pragma unroll
            for (int s = 0; s < 3; ++s) lds_w32(osw + OO + qt * 3072 + s * 1024, o[qt][s]);
        if (m == it) {
#pragma unroll
            for (int s = 0; s < 3; ++s) lds_w32(oLw + s * 1024, o[3][s]);
        }
        if (it == 0) WAIT_VM(0);
        arrive(cnt_addr, (it == 0 ? 0x1000u : 0x100000u) << (4 * p));
    };
    auto out_store = [&](const f32x4& c0, const f32x4& c1, uint32_t off) {
        u32x4 r = {pk(c0[0], c0[1]), pk(c0[2], c0[3]), pk(c1[0], c1[1]), pk(c1[2], c1[3])};
        gstore128(out, off, r);
    };

    Pre P;
    fetch(std::integral_constant<int, 0>{}, P);
    SCHED_FENCE();
    if (p == 0) {
        dma144((const char*)wo + 4608 * h, 16u * lane, Lbase + OFF_WO + 4608 * h);
        dma144((const char*)wo + 4608 * h + 2304, 16u * lane, Lbase + OFF_WO + 4608 * h + 2304);
    }
    window(std::integral_constant<int, 0>{}, P);
    PRIO_HOOK(2);
    proj_tile(xa + 2 * XS, F.Q[0], F.K[0], V0[0], V1[0]);
    proj_tile(xa + 2 * XS + 3072, F.Q[1], F.K[1], V0[1], V1[1]);
    proj_tile(xa + 2 * XS + 6144, F.Q[2], F.K[2], V0[2], V1[2]);
    set_vt();
    PRIO_HOOK(3);
    wait_fields(CNT, peek(CNT), p ? 0xFF000u : 0xF000u, p ? 0x44000u : 0x4000u);
    lds_ptr ord = (lds_ptr)(uintptr_t)((uint32_t)(uintptr_t)osw & ~15u);
    {
        u32x4 of[3][3], w[6];
        if (role < 3) {
#pragma unroll
            for (int s = 0; s < 3; ++s) {
                w[s] = wo_frag(0, s); w[3 + s] = wo_frag(8, s);
#pragma unroll
                for (int qt = 0; qt < 3; ++qt) of[qt][s] = lds_r128(ord + qt * 3072 + s * 1024);
            }
        }
        fetch(std::integral_constant<int, 1>{}, P);
        SCHED_FENCE();
        if (role < 3) {
            f32x4 c0[3], c1[3];
#pragma unroll
            for (int s = 0; s < 3; ++s)
#pragma unroll
                for (int qt = 0; qt < 3; ++qt) {
                    c0[qt] = mfma(w[s], of[qt][s], s ? c0[qt] : ZERO);
                    c1[qt] = mfma(w[3 + s], of[qt][s], s ? c1[qt] : ZERO);
                }
#pragma unroll
            for (int qt = 0; qt < 3; ++qt) out_store(c0[qt], c1[qt], so[qt]);
        }
    }
    PRIO_HOOK(4);
    window(std::integral_constant<int, 1>{}, P);
    PRIO_HOOK(5);
    if (role < 3) {
        uint32_t sx[3];
#pragma unroll
        for (int qt = 0; qt < 3; ++qt) sx[qt] = so[qt] + 2688u;
        if (ww0 + p + 2 == 31) {
            uint32_t mo = lane;
            asm volatile("" : "+v"(mo));
            mo &= 15u;
#pragma unroll
            for (int qt = 0; qt < 3; ++qt) {
                const uint32_t t = 16u * qt + mo, a = (t * 37u) >> 8;
                sx[qt] += (t - 7u * a >= 4u) ? (uint32_t)-43008 : 0u;
            }
        }
        u32x4 w[6];
#pragma unroll
        for (int s = 0; s < 3; ++s) { w[s] = wo_frag(0, s); w[3 + s] = wo_frag(8, s); }
        wait_fields(CNT, peek(CNT), 0xF00000u << (4 * p), 0x400000u << (4 * p));
        u32x4 of[3][3];
#pragma unroll
        for (int s = 0; s < 3; ++s)
#pragma unroll
            for (int qt = 0; qt < 3; ++qt) of[qt][s] = lds_r128(ord + 2 * OBS + qt * 3072 + s * 1024);
        f32x4 c0[3], c1[3];
#pragma unroll
        for (int s = 0; s < 3; ++s)
#pragma unroll
            for (int qt = 0; qt < 3; ++qt) {
                c0[qt] = mfma(w[s], of[qt][s], s ? c0[qt] : ZERO);
                c1[qt] = mfma(w[3 + s], of[qt][s], s ? c1[qt] : ZERO);
            }
#pragma unroll
        for (int qt = 0; qt < 3; ++qt) out_store(c0[qt], c1[qt], sx[qt]);
    } else {
        u32x4 w[3][6];
#pragma unroll
        for (int i2 = 0; i2 < 3; ++i2)
#pragma unroll
            for (int s = 0; s < 3; ++s) { w[i2][s] = wo_frag(64 * i2, s); w[i2][3 + s] = wo_frag(64 * i2 + 8, s); }
        int y = 7 * wh + 9; if (y >= 224) y -= 224;
        int xx = 7 * (ww0 + p + 2 * (m & 1)) + 9; if (xx >= 224) xx -= 224;
        const uint32_t dst = (uint32_t)((y * 224 + xx) * 192 + 16 * g);
        wait_fields(CNT, peek(CNT), 0xF00000u << (4 * p), 0x400000u << (4 * p));
        lds_ptr orr = L + OFF_OL + p * 3072 + lane * 16;
        u32x4 of0 = lds_r128(orr), of1 = lds_r128(orr + 1024), of2 = lds_r128(orr + 2048);
        f32x4 c0[3], c1[3];
#pragma unroll
        for (int i2 = 0; i2 < 3; ++i2) { c0[i2] = mfma(w[i2][0], of0, ZERO); c1[i2] = mfma(w[i2][3], of0, ZERO); }
#pragma unroll
        for (int i2 = 0; i2 < 3; ++i2) { c0[i2] = mfma(w[i2][1], of1, c0[i2]); c1[i2] = mfma(w[i2][4], of1, c1[i2]); }
#pragma unroll
        for (int i2 = 0; i2 < 3; ++i2) { c0[i2] = mfma(w[i2][2], of2, c0[i2]); c1[i2] = mfma(w[i2][5], of2, c1[i2]); }
        if (m < 2) {
#pragma unroll
            for (int i2 = 0; i2 < 3; ++i2) out_store(c0[i2], c1[i2], dst + 64 * i2);
        }
    }
    asm volatile("s_waitcnt vmcnt(0)" ::: "memory");
    { unsigned long long t1_ = __builtin_amdgcn_s_memrealtime(); unsigned long long dl_ = 0ull * tz1_; while (__builtin_amdgcn_s_memrealtime() - t1_ < dl_) { __builtin_amdgcn_s_sleep(1); } }
}

extern "C" void kernel_launch(void* const* d_in, const int* in_sizes, int n_in, void* d_out, int out_size, void* d_ws,
                              size_t ws_size, hipStream_t stream) {
    (void)in_sizes; (void)n_in; (void)out_size; (void)d_ws; (void)ws_size;
    (void)hipFuncSetAttribute((const void*)wattn_kernel, hipFuncAttributeMaxDynamicSharedMemorySize, LDS_TOTAL);
    wattn_kernel<<<256, 512, LDS_TOTAL, stream>>>((const uint16_t*)d_in[0], (const uint16_t*)d_in[1], (const uint16_t*)d_in[2],
                                                  (const uint16_t*)d_in[3], (const int*)d_in[4], (const uint16_t*)d_in[5],
                                                  (uint16_t*)d_out);
}
```

```cpp
#include <hip/hip_runtime.h>
#include <hip/hip_bf16.h>
#include <stdint.h>
#include <type_traits>

typedef __attribute__((ext_vector_type(8))) __bf16 bf16x8;
typedef __attribute__((ext_vector_type(2))) __bf16 bf16x2;
typedef __attribute__((ext_vector_type(4))) float f32x4;
typedef __attribute__((ext_vector_type(2))) float f32x2;
typedef __attribute__((ext_vector_type(4))) uint32_t u32x4;
typedef __attribute__((ext_vector_type(2))) uint32_t u32x2;
typedef __attribute__((ext_vector_type(4))) short s16x4;

#define DEV __device__ __forceinline__
#define LAS __attribute__((address_space(3)))
typedef LAS char* lds_ptr;

constexpr int OFF_W = 0;
constexpr int OFF_OB = 0, OBS = 9216;
constexpr int OFF_OL = 36864;
constexpr int OFF_RPB = 55296;
constexpr int OFF_X = 58016, XS = 9216;
constexpr int OFF_XL = OFF_X + 4 * XS;
constexpr int OFF_MASK = (OFF_XL + 768 + 127) / 128 * 128, MS = 6272;
constexpr int OFF_WO = OFF_MASK + 4 * MS;
constexpr int OFF_BH = OFF_WO;
constexpr int OFF_QL = OFF_BH + 36864;
constexpr int OFF_PL = OFF_QL + 2048;
constexpr int OFF_BL = OFF_PL + 1024;
constexpr int OFF_CNT = OFF_BL + 1024;
constexpr int OFF_DUMMY = OFF_CNT + 64;
constexpr int LDS_TOTAL = OFF_DUMMY + 256;
static_assert(LDS_TOTAL <= 163840 - 1024, "LDS");
static_assert(OFF_X % 16 == 0 && OFF_MASK % 128 == 0 && MS % 128 == 0 && OFF_WO % 16 == 0 && OFF_QL % 16 == 0, "align");

constexpr float LOG2E = 1.4426950408889634f;
constexpr float QSCALE = 0.20412414523193154f * 1.4426950408889634f;
constexpr float NEG_BIG = -1.0e30f;

DEV uint32_t pk(float a, float b) {
    f32x2 v = {a, b};
    bf16x2 r = __builtin_convertvector(v, bf16x2);
    return __builtin_bit_cast(uint32_t, r);
}
DEV uint32_t pks(float a, float b, float s) { return pk(a * s, __builtin_fmaf(b, s, 0.0f)); }
DEV f32x4 mfma(u32x4 a, u32x4 b, f32x4 c) {
    return __builtin_amdgcn_mfma_f32_16x16x32_bf16(__builtin_bit_cast(bf16x8, a), __builtin_bit_cast(bf16x8, b), c, 0, 0, 0);
}
DEV u32x4 lds_r128(lds_ptr p) { return *(const LAS u32x4*)p; }
DEV f32x4 lds_rf128(lds_ptr p) { return *(const LAS f32x4*)p; }
DEV u32x2 lds_r64(lds_ptr p) { return *(const LAS u32x2*)p; }
DEV uint32_t lds_r32(lds_ptr p) { return *(const LAS uint32_t*)p; }
DEV float lds_rf(lds_ptr p) { return *(const LAS float*)p; }
DEV uint32_t lds_r16(lds_ptr p) { return (uint32_t) * (const LAS uint16_t*)p; }
DEV void lds_w128(lds_ptr p, u32x4 v) { *(LAS u32x4*)p = v; }
DEV void lds_w32(lds_ptr p, uint32_t v) { *(LAS uint32_t*)p = v; }
DEV void lds_w16(lds_ptr p, uint32_t v) { *(LAS uint16_t*)p = (uint16_t)v; }
DEV u32x2 lds_tr(lds_ptr p) {
    return __builtin_bit_cast(u32x2, __builtin_amdgcn_ds_read_tr16_b64_v4i16((LAS s16x4*)p));
}
DEV float bflo(uint32_t v) { return __builtin_bit_cast(float, v << 16); }
DEV float bfhi(uint32_t v) { return __builtin_bit_cast(float, v & 0xffff0000u); }
DEV float ex2(float v) { return __builtin_amdgcn_exp2f(v); }

DEV void dma16nt(const void* sbase, uint32_t voff, uint32_t lds_dst) {
    unsigned keep;
    asm volatile("s_mov_b32 %0, m0\n\ts_mov_b32 m0, %3\n\ts_nop 0\n\tglobal_load_lds_dwordx4 %1, %2 nt\n\ts_mov_b32 m0, %0"
                 : "=&s"(keep) : "v"(voff), "s"(sbase), "s"(lds_dst) : "memory");
}
DEV void dma144(const void* sbase, uint32_t voff, uint32_t lds_dst) {
    unsigned keep;
    asm volatile("s_mov_b32 %0, m0\n\ts_mov_b32 m0, %3\n\ts_nop 0\n\t"
                 "global_load_lds_dwordx4 %1, %2\n\t"
                 "global_load_lds_dwordx4 %1, %2 offset:1024\n\t"
                 "s_mov_b64 exec, 0xffff\n\t"
                 "global_load_lds_dwordx4 %1, %2 offset:2048\n\t"
                 "s_mov_b64 exec, -1\n\ts_mov_b32 m0, %0"
                 : "=&s"(keep) : "v"(voff), "s"(sbase), "s"(lds_dst) : "memory");
}
DEV void dma144nt(const void* sbase, uint32_t v0, uint32_t v1, uint32_t v2, uint32_t lds_dst) {
    unsigned keep;
    asm volatile("s_mov_b32 %0, m0\n\ts_mov_b32 m0, %5\n\ts_nop 0\n\t"
                 "global_load_lds_dwordx4 %1, %4 nt\n\t"
                 "global_load_lds_dwordx4 %2, %4 offset:1024 nt\n\t"
                 "s_mov_b64 exec, 0xffff\n\t"
                 "global_load_lds_dwordx4 %3, %4 offset:2048 nt\n\t"
                 "s_mov_b64 exec, -1\n\ts_mov_b32 m0, %0"
                 : "=&s"(keep) : "v"(v0), "v"(v1), "v"(v2), "s"(sbase), "s"(lds_dst) : "memory");
}
DEV void touch(const void* sbase, uint32_t voff, uint32_t lds_dst) {
    unsigned keep;
    asm volatile("s_mov_b32 %0, m0\n\ts_mov_b32 m0, %3\n\ts_nop 0\n\tglobal_load_lds_dword %1, %2\n\ts_mov_b32 m0, %0"
                 : "=&s"(keep) : "v"(voff), "s"(sbase), "s"(lds_dst) : "memory");
}
DEV u32x4 gload128(const void* sbase, uint32_t voff) {
    u32x4 v;
    asm volatile("global_load_dwordx4 %0, %1, %2" : "=v"(v) : "v"(voff), "s"(sbase) : "memory");
    return v;
}
DEV uint32_t gload32(const void* sbase, uint32_t voff) {
    uint32_t v;
    asm volatile("global_load_dword %0, %1, %2" : "=v"(v) : "v"(voff), "s"(sbase) : "memory");
    return v;
}
DEV void gstore128(void* sbase, uint32_t voff, u32x4 v) {
    asm volatile("global_store_dwordx4 %0, %1, %2 sc0 sc1\n\ts_nop 1" :: "v"(voff), "v"(v), "s"(sbase) : "memory");
}
#define BARRIER() do { asm volatile("s_waitcnt lgkmcnt(0)" ::: "memory"); __builtin_amdgcn_s_barrier(); asm volatile("" ::: "memory"); } while (0)
#define WAIT_VM(n) asm volatile("s_waitcnt vmcnt(" #n ")" ::: "memory")

DEV void arrive(uint32_t cnt_addr, uint32_t inc) {
    uint32_t t0, t1;
    asm volatile("v_mov_b32 %0, %2\n\tv_mov_b32 %1, %3\n\ts_mov_b64 exec, 1\n\t"
                 "ds_add_u32 %0, %1\n\ts_mov_b64 exec, -1"
                 : "=&v"(t0), "=&v"(t1) : "s"(cnt_addr), "s"(inc) : "memory");
}
DEV uint32_t peek(lds_ptr cnt) { return __hip_atomic_load((LAS uint32_t*)cnt, __ATOMIC_RELAXED, __HIP_MEMORY_SCOPE_WORKGROUP); }
DEV void wait_fields(lds_ptr cnt, uint32_t v, uint32_t mask, uint32_t target) {
    while (((uint32_t)__builtin_amdgcn_readfirstlane(v) & mask) != target) v = peek(cnt);
    asm volatile("" ::: "memory");
}

struct Frags {
    u32x4 Q[3], K[4];
    u32x4 VT[2][2];
};

DEV void pv_norm(const Frags& F, u32x4 P0, u32x4 P1, uint32_t (&o)[3], uint32_t& sumbits) {
    f32x4 zero = {0.f, 0.f, 0.f, 0.f};
    f32x4 oa = mfma(F.VT[0][0], P0, zero);
    f32x4 ob = mfma(F.VT[1][0], P0, zero);
    oa = mfma(F.VT[0][1], P1, oa);
    ob = mfma(F.VT[1][1], P1, ob);
    float sum = ob[2];
    sumbits = __builtin_bit_cast(uint32_t, sum);
    float rs = __builtin_amdgcn_rcpf(sum);
    o[0] = pks(oa[0], oa[1], rs);
    o[1] = pks(oa[2], oa[3], rs);
    o[2] = pks(ob[0], ob[1], rs);
}

struct Sc { f32x4 s0, s1, s2; float s30; };
DEV void scores(const Frags& F, u32x4 Q, const f32x4 (&bias)[3], f32x4 CW, const u32x4 (&mk)[3], u32x4 IHL, Sc& S) {
    S.s0 = mfma(F.K[0], Q, bias[0]);
    S.s1 = mfma(F.K[1], Q, bias[1]);
    S.s2 = mfma(F.K[2], Q, bias[2]);
    f32x4 s3 = mfma(F.K[3], Q, CW);
    S.s30 = s3[0];
    S.s0 = mfma(IHL, mk[0], S.s0);
    S.s1 = mfma(IHL, mk[1], S.s1);
    S.s2 = mfma(IHL, mk[2], S.s2);
}
DEV void exp_pv(const Frags& F, const Sc& S, uint32_t (&o)[3], uint32_t& sumbits) {
    u32x4 P0 = {pk(ex2(S.s0[0]), ex2(S.s0[1])), pk(ex2(S.s0[2]), ex2(S.s0[3])), pk(ex2(S.s1[0]), ex2(S.s1[1])), pk(ex2(S.s1[2]), ex2(S.s1[3]))};
    u32x4 P1 = {pk(ex2(S.s2[0]), ex2(S.s2[1])), pk(ex2(S.s2[2]), ex2(S.s2[3])), pk(ex2(S.s30), 0.f), 0u};
    pv_norm(F, P0, P1, o, sumbits);
}
DEV void scores_safe(const Frags& F, u32x4 Q, const f32x4 (&bias)[3], f32x4 CW, lds_ptr M, Sc& S) {
    const int lane = threadIdx.x & 63;
    const int g = lane >> 4, mm = lane & 15;
    const int v = (mm >> 1) & 7;
    f32x4 zero = {0.f, 0.f, 0.f, 0.f};
    f32x4 s0 = mfma(F.K[0], Q, bias[0]);
    f32x4 s1 = mfma(F.K[1], Q, bias[1]);
    f32x4 s2 = mfma(F.K[2], Q, bias[2]);
    f32x4 s3 = mfma(F.K[3], Q, zero);
    const int h4 = 4 * (__builtin_amdgcn_readfirstlane(threadIdx.x >> 6) & 3);
    const uint32_t bb = lds_r32(M + 128 * mm + 16 * (7 ^ v) + h4);
    const float m48 = bflo(lds_r16(M + 128 * mm + 16 * (6 ^ v) + 14));
    float s30 = __builtin_fmaf(m48, LOG2E, s3[0] + (bflo(bb) + bfhi(bb)));
    u32x2 mv0 = lds_r64(M + 128 * mm + 16 * ((0 + (g >> 1)) ^ v) + 8 * (g & 1));
    u32x2 mv1 = lds_r64(M + 128 * mm + 16 * ((2 + (g >> 1)) ^ v) + 8 * (g & 1));
    u32x2 mv2 = lds_r64(M + 128 * mm + 16 * ((4 + (g >> 1)) ^ v) + 8 * (g & 1));
#define ADDM(s, mv) s[0] = __builtin_fmaf(bflo(mv[0]), LOG2E, s[0]); s[1] = __builtin_fmaf(bfhi(mv[0]), LOG2E, s[1]); \
                    s[2] = __builtin_fmaf(bflo(mv[1]), LOG2E, s[2]); s[3] = __builtin_fmaf(bfhi(mv[1]), LOG2E, s[3]);
    ADDM(s0, mv0) ADDM(s1, mv1) ADDM(s2, mv2)
#undef ADDM
    s30 = (CW[0] == 0.f) ? s30 : -INFINITY;
    float mx = s30;
#pragma unroll
    for (int r = 0; r < 4; ++r) mx = fmaxf(mx, fmaxf(s0[r], fmaxf(s1[r], s2[r])));
    mx = fmaxf(mx, __shfl_xor(mx, 16));
    mx = fmaxf(mx, __shfl_xor(mx, 32));
#pragma unroll
    for (int r = 0; r < 4; ++r) { s0[r] -= mx; s1[r] -= mx; s2[r] -= mx; }
    S.s0 = s0; S.s1 = s1; S.s2 = s2; S.s30 = s30 - mx;
}

template <bool SAFE>
DEV void left_scores(const Frags& F, float cinit, bool valid, u32x4 ql, lds_ptr PLw) {
    const int g = (threadIdx.x & 63) >> 4;
    const f32x4 c = {cinit, 0.f, 0.f, 0.f};
    const f32x4 s0 = mfma(ql, F.K[0], c), s1 = mfma(ql, F.K[1], c), s2 = mfma(ql, F.K[2], c), s3 = mfma(ql, F.K[3], c);
    const float va = (g & 1) ? s1[0] : s0[0], vb = (g & 1) ? s3[0] : s2[0];
    float v = (g & 2) ? vb : va;
    if (SAFE) {
        v = valid ? v : -INFINITY;
        float mx = v;
#pragma unroll
        for (int d = 1; d < 64; d <<= 1) mx = fmaxf(mx, __shfl_xor(mx, d));
        v -= mx;
    }
    lds_w16(PLw, pk(ex2(v), 0.f));
}
struct PLd { u32x2 a0, a1, a2, a3; };
DEV PLd left_ld(lds_ptr PLr) { return PLd{lds_r64(PLr), lds_r64(PLr + 32), lds_r64(PLr + 64), lds_r64(PLr + 96)}; }
DEV void left_pv(const Frags& F, const PLd& a, uint32_t (&o)[3], uint32_t& sumbits) {
    u32x4 P0 = {a.a0[0], a.a0[1], a.a1[0], a.a1[1]}, P1 = {a.a2[0], a.a2[1], a.a3[0], a.a3[1]};
    pv_norm(F, P0, P1, o, sumbits);
}
#define PR0 -1
#define PR1 1
#define PR0B -1
#define PR1B 5
#define PRIO_HOOK(k) do { if (PR1 == k) { if (p == 1) __builtin_amdgcn_s_setprio(1); } if (PR1B == k) { if (p == 1) __builtin_amdgcn_s_setprio(0); } \
    if (PR0 == k) { if (p == 0) __builtin_amdgcn_s_setprio(2); } if (PR0B == k) { if (p == 0) __builtin_amdgcn_s_setprio(0); } } while (0)
#define SCHED_FENCE() __builtin_amdgcn_sched_barrier(0)
#define NPRE0 2

__global__ __launch_bounds__(512, 2) void wattn_kernel(const uint16_t* __restrict__ x, const uint16_t* __restrict__ wqkv,
                                                       const uint16_t* __restrict__ wo, const uint16_t* __restrict__ rpb,
                                                       const int* __restrict__ rpidx, const uint16_t* __restrict__ mask,
                                                       uint16_t* __restrict__ out) {
    extern __shared__ __attribute__((aligned(16))) char smem_[];
    lds_ptr L = (lds_ptr)smem_;
    const uint32_t Lbase = (uint32_t)(uintptr_t)L;
    const int tid = threadIdx.x, lane = tid & 63;
    const int wv = __builtin_amdgcn_readfirstlane(tid >> 6);
    {
        uint64_t pc;
        asm volatile("s_getpc_b64 %0" : "=s"(pc));
        touch((const void*)pc, 32u * (uint32_t)tid + 1024u, Lbase + OFF_DUMMY);
    }
    const int h = wv & 3, p = wv >> 2;
    const int g = lane >> 4, m = lane & 15;
    const int blk = blockIdx.x;
    const int wh = blk >> 3, ww0 = (blk & 7) * 4;
    const uint32_t wrapY = (wh == 31) ? (uint32_t)-9633792 : 0u;
    const uint32_t org0 = (uint32_t)(((7 * wh + 3) * 224 + 7 * ww0 + 3) * 192);
    const int role = (h + 2 * p) & 3;

    const uint32_t rsrc = 4 * (tid < 338 ? tid : 337);
    const uint32_t l48 = lane < 48 ? lane : 48;
    const uint32_t ilo = 196u * m + 16u * g;
    const int fq = (wv * 11) >> 5;
    const uint32_t isrcA = ilo + (uint32_t)(3136 * fq + 64 * (wv - 3 * fq));
    const uint32_t isrcB = (wv == 4) ? ilo + 6400u : (wv == 5) ? 196u * l48 + 192u : 9408u + 4u * l48;
    uint32_t xr[3];
#pragma unroll
    for (int j = 0; j < 3; ++j) {
        const uint32_t pos = 144u * h + 64u * j + lane;
        const uint32_t a = (pos * 781u) >> 16, p96 = (pos * 683u) >> 16;
        xr[j] = ((pos << 4) ^ ((p96 & 1u) << 5)) + 41664u * a + (pos >= 336u ? wrapY : 0u) + (org0 + 1344u * p + 8192u - 1024u * j);
    }
    const uint32_t wvoff = (16u * lane) ^ (uint32_t)(32 * ((wv >> 1) & 1));
    uint32_t msrc[2];
    bool mok[2];
#pragma unroll
    for (int j = 0; j < 2; ++j) {
        const int pos = 98 * h + 64 * j + lane;
        const int q = pos >> 3, mk = (pos & 7) ^ ((q >> 1) & 7);
        msrc[j] = 98 * q + (mk < 6 ? 16 * mk : 82);
        mok[j] = (mk < 7) && (j == 0 || lane < 34);
    }
    if (wv == 7 && lane == 0) lds_w32(L + OFF_CNT, 0u);
    asm volatile("" :: "v"(rsrc), "v"(isrcA), "v"(isrcB), "v"(xr[0]), "v"(xr[1]), "v"(xr[2]), "v"(wvoff), "v"(msrc[0]), "v"(msrc[1]));
    const float onec = ((m & 3) >= 2) ? 1.0f : 0.0f;
    const f32x4 ONES = {onec, onec, onec, onec};
    const f32x4 ZERO = {0.f, 0.f, 0.f, 0.f};
    const float qsc = (g & 1) ? 1.0f : QSCALE;
    lds_ptr QL = L + OFF_QL + wv * 256;
    lds_ptr CNT = L + OFF_CNT;
    const uint32_t cnt_addr = Lbase + OFF_CNT;
    lds_ptr wab, wc, wv1;
    {
        const int q4 = m >> 2, p4 = m & 3, sx = 32 * (g & 1);
        lds_ptr rb = L + OFF_W + 576 * (8 * g + q4);
        wab = rb + ((48 * h + 8 * p4) ^ sx);
        wc = rb + ((48 * h + 32 + 8 * (p4 >> 1)) ^ sx) + 192 * (p4 & 1);
        wv1 = rb + 384 + ((48 * h + 32 + 8 * (p4 & 1)) ^ sx);
    }
    const int bperm = 4 * (16 * g + 2 * (m >> 2) + (m & 1));
    const uint32_t bmask = ((m & 3) < 2) ? 0xffffffffu : 0u;
    u32x4 IHL;
    {
        const uint32_t val = (g < 2) ? 0x3FB9u : 0xBB2Cu;
        const int j = m - 8 * (g & 1);
#pragma unroll
        for (int r = 0; r < 4; ++r) IHL[r] = (j == 2 * r) ? val : (j == 2 * r + 1) ? (val << 16) : 0u;
    }
    lds_ptr q3a, mLa, qa, osw, oLw, xa, woa0, woa2, PLw, PLr;
    uint32_t ma0;
    {
        const int vsw = (m >> 1) & 7;
        lds_ptr M = L + OFF_MASK + p * MS;
        ma0 = Lbase + OFF_MASK + p * MS + 128 * m + 16 * ((g & 1) ^ vsw);
        q3a = M + 128 * m + (g < 2 ? 16 * (6 ^ vsw) + 12 : 16 * (7 ^ vsw) + 4 * h);
        mLa = M + 128 * 48 + (lane < 48 ? 2 * lane : 110);
        qa = QL + 64 * p + 16 * g;
        osw = L + OFF_OB + p * OBS + lane * 16 + 4 * (h ^ (g & 1));
        oLw = L + OFF_OL + p * 3072 + lane * 16 + 4 * (h ^ (g & 1));
        xa = L + OFF_X + p * XS + 192 * m + ((16 * g) ^ (((m >> 3) & 1) << 5));
        const int q4 = m >> 2, p4 = m & 3;
        const int rb = 24 * ((q4 >> 1) ^ (g & 1)) + (q4 & 1);
        const int cb = (role < 3) ? 64 * role : 0;
        woa0 = L + OFF_WO + (rb + 4 * g) * 192 + 16 * p4 + cb;
        woa2 = L + OFF_WO + (rb + 16 + 2 * g) * 192 + 16 * p4 + cb;
        PLw = L + OFF_PL + wv * 128 + 2 * lane;
        PLr = L + OFF_PL + wv * 128 + 8 * g;
    }
    uint32_t so[3];
#pragma unroll
    for (int qt = 0; qt < 3; ++qt) {
        const uint32_t t = 16u * qt + m, a = (t * 37u) >> 8;
        so[qt] = 192u * t + 41664u * a + (a >= 4u ? wrapY : 0u) + 16u * g + (uint32_t)(64 * role) + (org0 + 1344u * p);
    }
    asm volatile("" : "+v"(so[0]), "+v"(so[1]), "+v"(so[2]), "+v"(IHL), "+v"(ma0), "+v"(q3a), "+v"(mLa));
    asm volatile("" : "+v"(qa), "+v"(osw), "+v"(xa), "+v"(woa0), "+v"(woa2));
    asm volatile("" : "+v"(wab), "+v"(wc), "+v"(wv1));

    const char* xm = (const char*)x - 8192;
    auto load_w = [&](int s) {
        dma144((const char*)wqkv + 18432 * s + 2304 * wv, wvoff, Lbase + OFF_W + 18432 * s + 2304 * wv);
    };
    auto load_mask = [&](int i) {
        const char* mb = (const char*)mask + (size_t)(4 * blk + i) * 4802;
        const uint32_t dst = Lbase + OFF_MASK + i * MS + 98 * 16 * h;
        if (mok[0]) dma16nt(mb, msrc[0], dst);
        if (mok[1]) dma16nt(mb, msrc[1], dst + 1024);
    };
    u32x4 ivA, ivB = {0u, 0u, 0u, 0u};
    if (wv == 4) ivB = gload128(rpidx, isrcB);
    else if (wv == 5 || wv == 6) ivB[0] = gload32(rpidx, isrcB);
    uint32_t rpbv = gload32(rpb, rsrc);
    ivA = gload128(rpidx, isrcA);
    {
        int y = 7 * wh + 9; if (y >= 224) y -= 224;
        int xx = 7 * (ww0 + (wv >> 1)) + 9; if (xx >= 224) xx -= 224;
        if (lane < 6) dma16nt(x, (uint32_t)((y * 224 + xx) * 192 + 96 * (wv & 1) + 16 * lane), Lbase + OFF_XL + 96 * wv);
    }
    dma144nt(xm, xr[0], xr[1], xr[2], Lbase + OFF_X + p * XS + 2304 * h);
    load_w(0);
    load_w(1);

    asm volatile("s_waitcnt vmcnt(11)" : "+v"(rpbv), "+v"(ivB) :: "memory");
    if (tid < 338) *(LAS f32x2*)(L + OFF_RPB + 8 * tid) = f32x2{bflo(rpbv) * LOG2E, bfhi(rpbv) * LOG2E};
    BARRIER();
    load_w(2);
    touch((const char*)mask + (size_t)blk * 19208, min(40u * (uint32_t)tid, 19204u), Lbase + OFF_DUMMY);
    auto bias_unit = [&](int f, u32x4 iv) {
        lds_ptr T = L + OFF_RPB;
        const u32x4 b0 = lds_r128(T + 16 * iv[0]), b1 = lds_r128(T + 16 * iv[1]), b2 = lds_r128(T + 16 * iv[2]), b3 = lds_r128(T + 16 * iv[3]);
        lds_ptr D = L + OFF_BH + 1024 * f + 16 * lane;
#pragma unroll
        for (int hb = 0; hb < 4; ++hb) lds_w128(D + 9216 * hb, u32x4{b0[hb], b1[hb], b2[hb], b3[hb]});
    };
    asm volatile("s_waitcnt vmcnt(14)" : "+v"(ivA) :: "memory");
    bias_unit(wv, ivA);
    if (wv == 4) bias_unit(8, ivB);
    else if (wv == 5) {
        const f32x4 b48 = lds_rf128(L + OFF_RPB + 16 * ivB[0]);
        u32x4 pr;
#pragma unroll
        for (int hb = 0; hb < 4; ++hb) {
            const uint32_t hi = pk(b48[hb], 0.f) & 0xffffu;
            pr[hb] = hi | (pk(b48[hb] - bflo(hi), 0.f) << 16);
        }
        const int a48 = 128 * lane + 16 * (7 ^ ((lane >> 1) & 7));
        if (lane < 48) {
#pragma unroll
            for (int i = 0; i < 4; ++i) lds_w128(L + OFF_MASK + i * MS + a48, pr);
        }
    } else if (wv == 6) lds_w128(L + OFF_BL + 16 * lane, lds_r128(L + OFF_RPB + 16 * ivB[0]));
    WAIT_VM(1);
    BARRIER();
    PRIO_HOOK(0);

    u32x4 Wf[5][3];
    auto load_wf = [&](int s) {
        auto fr = [&](lds_ptr a) -> u32x4 {
            u32x2 lo = lds_tr(a + 18432 * s), hi = lds_tr(a + 18432 * s + 2304);
            return u32x4{lo[0], lo[1], hi[0], hi[1]};
        };
        Wf[0][s] = fr(wab);
        Wf[1][s] = fr(wab + 192);
        Wf[2][s] = fr(wc);
        Wf[3][s] = fr(wab + 384);
        const u32x4 t = fr(wv1);
#pragma unroll
        for (int r = 0; r < 4; ++r) Wf[4][s][r] = (uint32_t)__builtin_amdgcn_ds_bpermute(bperm, (int)t[r]) & bmask;
    };
    struct Acc { f32x4 a0, a1, a2, v0, v1; };
    auto proj_step = [&](Acc& A, u32x4 xf, int s, bool first) {
        A.a0 = mfma(Wf[0][s], xf, first ? ZERO : A.a0);
        A.a1 = mfma(Wf[1][s], xf, first ? ZERO : A.a1);
        A.a2 = mfma(Wf[2][s], xf, first ? ZERO : A.a2);
        A.v0 = mfma(xf, Wf[3][s], first ? ZERO : A.v0);
        A.v1 = mfma(xf, Wf[4][s], first ? ONES : A.v1);
    };
    auto proj_fin = [&](const Acc& A, u32x4& Q, u32x4& K, u32x2& V0, u32x2& V1) {
        const uint32_t c0 = pks(A.a2[0], A.a2[1], qsc), c1 = pks(A.a2[2], A.a2[3], qsc);
        const auto sw = __builtin_amdgcn_permlane16_swap(c0, c1, false, false);
        Q[0] = pks(A.a0[0], A.a0[1], QSCALE); Q[1] = pks(A.a0[2], A.a0[3], QSCALE); Q[2] = sw[0];
        K[0] = pk(A.a1[0], A.a1[1]); K[1] = pk(A.a1[2], A.a1[3]); K[2] = sw[1];
        V0 = u32x2{pk(A.v0[0], A.v0[1]), pk(A.v0[2], A.v0[3])};
        V1 = u32x2{pk(A.v1[0], A.v1[1]), pk(A.v1[2], A.v1[3])};
    };
    Frags F;
    F.Q[0][3] = F.Q[1][3] = F.Q[2][3] = 0u;
    F.K[0][3] = F.K[1][3] = F.K[2][3] = 0u;
    F.K[3][3] = (g == 0) ? 0x3FB90000u : (g == 1) ? 0xBB2C0000u : (g == 2) ? 0x3F803F80u : 0u;
    u32x2 V0L, V1L;
    f32x4 bias[3][3];
    float biasL;
    u32x2 V0[3], V1[3];
    auto set_vt = [&]() {
        F.VT[0][0] = u32x4{V0[0][0], V0[0][1], V0[1][0], V0[1][1]};
        F.VT[0][1] = u32x4{V0[2][0], V0[2][1], V0L[0], V0L[1]};
        F.VT[1][0] = u32x4{V1[0][0], V1[0][1], V1[1][0], V1[1][1]};
        F.VT[1][1] = u32x4{V1[2][0], V1[2][1], V1L[0], V1L[1]};
    };
    auto proj_tile = [&](lds_ptr a, u32x4& Q, u32x4& K, u32x2& v0, u32x2& v1) {
        u32x4 xf0 = lds_r128(a), xf1 = lds_r128(a + 64), xf2 = lds_r128(a + 128);
        Acc A;
        proj_step(A, xf0, 0, true); proj_step(A, xf1, 1, false); proj_step(A, xf2, 2, false);
        proj_fin(A, Q, K, v0, v1);
    };

    {
        lds_ptr xl = L + OFF_XL + 192 * (m >> 2) + 16 * g;
        const u32x4 xl0 = lds_r128(xl), xl1 = lds_r128(xl + 64), xl2 = lds_r128(xl + 128);
        load_wf(0); load_wf(1); load_wf(2);
        const u32x4 x00 = lds_r128(xa), x01 = lds_r128(xa + 64), x02 = lds_r128(xa + 128);
        {
            lds_ptr B = L + OFF_BH + 9216 * h + 16 * lane;
#pragma unroll
            for (int qt = 0; qt < 3; ++qt)
#pragma unroll
                for (int kt = 0; kt < 3; ++kt) bias[qt][kt] = lds_rf128(B + 1024 * (3 * qt + kt));
            biasL = lds_rf(L + OFF_BL + 16 * lane + 4 * h);
        }
        load_mask(p);
        {
            uint32_t v0 = xr[0], v1 = xr[1], v2 = xr[2];
            if (p == 1 && ww0 == 28) {
    #pragma unroll
                for (int j = 0; j < 3; ++j) {
                    const uint32_t pos = 144u * h + 64u * j + lane;
                    const uint32_t a = (pos * 781u) >> 16;
                    const uint32_t d = (pos - 84u * a >= 48u) ? (uint32_t)-43008 : 0u;
                    if (j == 0) v0 += d; else if (j == 1) v1 += d; else v2 += d;
                }
            }
            dma144nt(xm + 2688, v0, v1, v2, Lbase + OFF_X + (p + 2) * XS + 2304 * h);
        }
        load_mask(p + 2);
        Acc A, B;
        proj_step(A, xl0, 0, true); proj_step(A, xl1, 1, false); proj_step(A, xl2, 2, false);
        arrive(cnt_addr, 1u);
        proj_step(B, x00, 0, true); proj_step(B, x01, 1, false); proj_step(B, x02, 2, false);
        u32x4 QLr = {0u, 0u, 0u, 0u};
        proj_fin(A, QLr, F.K[3], V0L, V1L);
        if ((m & 3) == 0) lds_w128(QL + (m >> 2) * 64 + g * 16, QLr);
        proj_fin(B, F.Q[0], F.K[0], V0[0], V1[0]);
        proj_tile(xa + 3072, F.Q[1], F.K[1], V0[1], V1[1]);
        proj_tile(xa + 6144, F.Q[2], F.K[2], V0[2], V1[2]);
        set_vt();
    }
    WAIT_VM(2);
    arrive(cnt_addr, 16u << (4 * p));
    PRIO_HOOK(1);
    wait_fields(CNT, peek(CNT), 0xFu | (0xF0u << (4 * p)), 8u | (0x40u << (4 * p)));

    auto wo_frag = [&](int colb, int s) -> u32x4 {
        lds_ptr a = (s == 2 ? woa2 : woa0 + (s == 1 ? 384 : 0)) + colb;
        u32x2 lo = lds_tr(a), hi = lds_tr(a + 48 * 192);
        return u32x4{lo[0], lo[1], hi[0], hi[1]};
    };
    struct Pre { u32x4 ql, mk[3][3]; uint32_t q3[3], mL; };
    auto fetch = [&](auto itc, Pre& P) {
        constexpr int it = decltype(itc)::value;
        constexpr int MO = 2 * MS * it;
        P.ql = lds_r128(qa + 128 * it);
        P.mL = lds_r16(mLa + MO);
        uint32_t a0 = ma0;
        asm volatile("" : "+v"(a0));
        lds_ptr ma0p = (lds_ptr)(uintptr_t)a0, ma1p = (lds_ptr)(uintptr_t)(a0 ^ 32u), ma2p = (lds_ptr)(uintptr_t)(a0 ^ 64u);
#pragma unroll
        for (int tt = 0; tt < 3; ++tt) P.q3[tt] = lds_r32(q3a + MO + 2048 * tt);
#pragma unroll
        for (int tt = 0; tt < (it == 0 ? NPRE0 : 3); ++tt) {
            P.mk[tt][0] = lds_r128(ma0p + MO + 2048 * tt);
            P.mk[tt][1] = lds_r128(ma1p + MO + 2048 * tt);
            P.mk[tt][2] = lds_r128(ma2p + MO + 2048 * tt);
        }
    };
    auto window = [&](auto itc, Pre& P) {
        constexpr int it = decltype(itc)::value;
        constexpr int OO = 2 * OBS * it;
        const int i = p + 2 * it;
        uint32_t o[4][3];
        const f32x4 CW = {(g == i) ? 0.f : NEG_BIG, 0.f, 0.f, 0.f};
        const bool validL = (lane < 48) || (lane == 48 + 4 * i);
        uint32_t sb[4];
        asm volatile("" : "+v"(P.mL));
        const float cL = validL ? __builtin_fmaf(bflo(P.mL), LOG2E, biasL) : NEG_BIG;
        auto check = [&]() -> bool {
            const uint32_t lo = min(min(sb[0], sb[1]), min(sb[2], sb[3])), hi = max(max(sb[0], sb[1]), max(sb[2], sb[3]));
            const bool bad = (lo < 0x2B800000u) || (hi > 0x53800000u);
            return __builtin_amdgcn_ballot_w64(bad) != 0ull;
        };
        {
#pragma unroll
            for (int tt = 0; tt < 3; ++tt) F.Q[tt][3] = P.q3[tt];
            Sc S0, S1, S2;
            left_scores<false>(F, cL, validL, P.ql, PLw);
            const PLd pl = left_ld(PLr);
            scores(F, F.Q[0], bias[0], CW, P.mk[0], IHL, S0);
            if (it == 0) {
                uint32_t a0 = ma0;
                asm volatile("" : "+v"(a0));
                lds_ptr ma0p = (lds_ptr)(uintptr_t)a0, ma1p = (lds_ptr)(uintptr_t)(a0 ^ 32u), ma2p = (lds_ptr)(uintptr_t)(a0 ^ 64u);
#pragma unroll
                for (int tt = NPRE0; tt < 3; ++tt) {
                    P.mk[tt][0] = lds_r128(ma0p + 2048 * tt);
                    P.mk[tt][1] = lds_r128(ma1p + 2048 * tt);
                    P.mk[tt][2] = lds_r128(ma2p + 2048 * tt);
                }
            }
            SCHED_FENCE();
            scores(F, F.Q[1], bias[1], CW, P.mk[1], IHL, S1);
            exp_pv(F, S0, o[0], sb[0]);
            scores(F, F.Q[2], bias[2], CW, P.mk[2], IHL, S2);
            exp_pv(F, S1, o[1], sb[1]);
            left_pv(F, pl, o[3], sb[3]);
            exp_pv(F, S2, o[2], sb[2]);
        }
        if (__builtin_expect(check(), 0)) {
            lds_ptr M = L + OFF_MASK + i * MS;
#pragma unroll
            for (int qt = 0; qt < 3; ++qt) {
                Sc S;
                u32x4 Q = F.Q[qt];
                Q[3] = 0u;
                scores_safe(F, Q, bias[qt], CW, M + 2048 * qt, S);
                exp_pv(F, S, o[qt], sb[qt]);
            }
            left_scores<true>(F, cL, validL, lds_r128(qa + 128 * it), PLw);
            left_pv(F, left_ld(PLr), o[3], sb[3]);
        }
#pragma unroll
        for (int qt = 0; qt < 3; ++qt)
#pragma unroll
            for (int s = 0; s < 3; ++s) lds_w32(osw + OO + qt * 3072 + s * 1024, o[qt][s]);
        if (m == it) {
#pragma unroll
            for (int s = 0; s < 3; ++s) lds_w32(oLw + s * 1024, o[3][s]);
        }
        if (it == 0) WAIT_VM(0);
        arrive(cnt_addr, (it == 0 ? 0x1000u : 0x100000u) << (4 * p));
    };
    auto out_store = [&](const f32x4& c0, const f32x4& c1, uint32_t off) {
        u32x4 r = {pk(c0[0], c0[1]), pk(c0[2], c0[3]), pk(c1[0], c1[1]), pk(c1[2], c1[3])};
        gstore128(out, off, r);
    };

    Pre P;
    fetch(std::integral_constant<int, 0>{}, P);
    SCHED_FENCE();
    if (p == 0) {
        dma144((const char*)wo + 4608 * h, 16u * lane, Lbase + OFF_WO + 4608 * h);
        dma144((const char*)wo + 4608 * h + 2304, 16u * lane, Lbase + OFF_WO + 4608 * h + 2304);
    }
    window(std::integral_constant<int, 0>{}, P);
    PRIO_HOOK(2);
    proj_tile(xa + 2 * XS, F.Q[0], F.K[0], V0[0], V1[0]);
    proj_tile(xa + 2 * XS + 3072, F.Q[1], F.K[1], V0[1], V1[1]);
    proj_tile(xa + 2 * XS + 6144, F.Q[2], F.K[2], V0[2], V1[2]);
    set_vt();
    PRIO_HOOK(3);
    wait_fields(CNT, peek(CNT), p ? 0xFF000u : 0xF000u, p ? 0x44000u : 0x4000u);
    lds_ptr ord = (lds_ptr)(uintptr_t)((uint32_t)(uintptr_t)osw & ~15u);
    {
        u32x4 of[3][3], w[6];
        if (role < 3) {
#pragma unroll
            for (int s = 0; s < 3; ++s) {
                w[s] = wo_frag(0, s); w[3 + s] = wo_frag(8, s);
#pragma unroll
                for (int qt = 0; qt < 3; ++qt) of[qt][s] = lds_r128(ord + qt * 3072 + s * 1024);
            }
        }
        fetch(std::integral_constant<int, 1>{}, P);
        SCHED_FENCE();
        if (role < 3) {
            f32x4 c0[3], c1[3];
#pragma unroll
            for (int s = 0; s < 3; ++s)
#pragma unroll
                for (int qt = 0; qt < 3; ++qt) {
                    c0[qt] = mfma(w[s], of[qt][s], s ? c0[qt] : ZERO);
                    c1[qt] = mfma(w[3 + s], of[qt][s], s ? c1[qt] : ZERO);
                }
#pragma unroll
            for (int qt = 0; qt < 3; ++qt) out_store(c0[qt], c1[qt], so[qt]);
        }
    }
    PRIO_HOOK(4);
    window(std::integral_constant<int, 1>{}, P);
    PRIO_HOOK(5);
    if (role < 3) {
        uint32_t sx[3];
#pragma unroll
        for (int qt = 0; qt < 3; ++qt) sx[qt] = so[qt] + 2688u;
        if (ww0 + p + 2 == 31) {
            uint32_t mo = lane;
            asm volatile("" : "+v"(mo));
            mo &= 15u;
#pragma unroll
            for (int qt = 0; qt < 3; ++qt) {
                const uint32_t t = 16u * qt + mo, a = (t * 37u) >> 8;
                sx[qt] += (t - 7u * a >= 4u) ? (uint32_t)-43008 : 0u;
            }
        }
        u32x4 w[6];
#pragma unroll
        for (int s = 0; s < 3; ++s) { w[s] = wo_frag(0, s); w[3 + s] = wo_frag(8, s); }
        wait_fields(CNT, peek(CNT), 0xF00000u << (4 * p), 0x400000u << (4 * p));
        u32x4 of[3][3];
#pragma unroll
        for (int s = 0; s < 3; ++s)
#pragma unroll
            for (int qt = 0; qt < 3; ++qt) of[qt][s] = lds_r128(ord + 2 * OBS + qt * 3072 + s * 1024);
        f32x4 c0[3], c1[3];
#pragma unroll
        for (int s = 0; s < 3; ++s)
#pragma unroll
            for (int qt = 0; qt < 3; ++qt) {
                c0[qt] = mfma(w[s], of[qt][s], s ? c0[qt] : ZERO);
                c1[qt] = mfma(w[3 + s], of[qt][s], s ? c1[qt] : ZERO);
            }
#pragma unroll
        for (int qt = 0; qt < 3; ++qt) out_store(c0[qt], c1[qt], sx[qt]);
    } else {
        u32x4 w[3][6];
#pragma unroll
        for (int i2 = 0; i2 < 3; ++i2)
#pragma unroll
            for (int s = 0; s < 3; ++s) { w[i2][s] = wo_frag(64 * i2, s); w[i2][3 + s] = wo_frag(64 * i2 + 8, s); }
        int y = 7 * wh + 9; if (y >= 224) y -= 224;
        int xx = 7 * (ww0 + p + 2 * (m & 1)) + 9; if (xx >= 224) xx -= 224;
        const uint32_t dst = (uint32_t)((y * 224 + xx) * 192 + 16 * g);
        wait_fields(CNT, peek(CNT), 0xF00000u << (4 * p), 0x400000u << (4 * p));
        lds_ptr orr = L + OFF_OL + p * 3072 + lane * 16;
        u32x4 of0 = lds_r128(orr), of1 = lds_r128(orr + 1024), of2 = lds_r128(orr + 2048);
        f32x4 c0[3], c1[3];
#pragma unroll
        for (int i2 = 0; i2 < 3; ++i2) { c0[i2] = mfma(w[i2][0], of0, ZERO); c1[i2] = mfma(w[i2][3], of0, ZERO); }
#pragma unroll
        for (int i2 = 0; i2 < 3; ++i2) { c0[i2] = mfma(w[i2][1], of1, c0[i2]); c1[i2] = mfma(w[i2][4], of1, c1[i2]); }
#pragma unroll
        for (int i2 = 0; i2 < 3; ++i2) { c0[i2] = mfma(w[i2][2], of2, c0[i2]); c1[i2] = mfma(w[i2][5], of2, c1[i2]); }
        if (m < 2) {
#pragma unroll
            for (int i2 = 0; i2 < 3; ++i2) out_store(c0[i2], c1[i2], dst + 64 * i2);
        }
    }
}

extern "C" void kernel_launch(void* const* d_in, const int* in_sizes, int n_in, void* d_out, int out_size, void* d_ws,
                              size_t ws_size, hipStream_t stream) {
    (void)in_sizes; (void)n_in; (void)out_size; (void)d_ws; (void)ws_size;
    (void)hipFuncSetAttribute((const void*)wattn_kernel, hipFuncAttributeMaxDynamicSharedMemorySize, LDS_TOTAL);
    wattn_kernel<<<256, 512, LDS_TOTAL, stream>>>((const uint16_t*)d_in[0], (const uint16_t*)d_in[1], (const uint16_t*)d_in[2],
                                                  (const uint16_t*)d_in[3], (const int*)d_in[4], (const uint16_t*)d_in[5],
                                                  (uint16_t*)d_out);
}
```

```cpp
#include <hip/hip_runtime.h>
#include <hip/hip_bf16.h>
#include <stdint.h>
#include <type_traits>

typedef __attribute__((ext_vector_type(8))) __bf16 bf16x8;
typedef __attribute__((ext_vector_type(2))) __bf16 bf16x2;
typedef __attribute__((ext_vector_type(4))) float f32x4;
typedef __attribute__((ext_vector_type(2))) float f32x2;
typedef __attribute__((ext_vector_type(4))) uint32_t u32x4;
typedef __attribute__((ext_vector_type(2))) uint32_t u32x2;
typedef __attribute__((ext_vector_type(4))) short s16x4;

#define DEV __device__ __forceinline__
#define LAS __attribute__((address_space(3)))
typedef LAS char* lds_ptr;

constexpr int OFF_W = 0;
constexpr int OFF_OB = 0, OBS = 9216;
constexpr int OFF_OL = 36864;
constexpr int OFF_RPB = 55296;
constexpr int OFF_X = 58016, XS = 9216;
constexpr int OFF_XL = OFF_X + 4 * XS;
constexpr int OFF_MASK = (OFF_XL + 768 + 127) / 128 * 128, MS = 6272;
constexpr int OFF_WO = OFF_MASK + 4 * MS;
constexpr int OFF_BH = OFF_WO;
constexpr int OFF_QL = OFF_BH + 36864;
constexpr int OFF_PL = OFF_QL + 2048;
constexpr int OFF_BL = OFF_PL + 1024;
constexpr int OFF_CNT = OFF_BL + 1024;
constexpr int LDS_TOTAL = OFF_CNT + 64;
static_assert(LDS_TOTAL <= 163840 - 1024, "LDS");
static_assert(OFF_X % 16 == 0 && OFF_MASK % 128 == 0 && MS % 128 == 0 && OFF_WO % 16 == 0 && OFF_QL % 16 == 0, "align");

constexpr float LOG2E = 1.4426950408889634f;
constexpr float QSCALE = 0.20412414523193154f * 1.4426950408889634f;
constexpr float NEG_BIG = -1.0e30f;

DEV uint32_t pk(float a, float b) {
    f32x2 v = {a, b};
    bf16x2 r = __builtin_convertvector(v, bf16x2);
    return __builtin_bit_cast(uint32_t, r);
}
DEV uint32_t pks(float a, float b, float s) { return pk(a * s, __builtin_fmaf(b, s, 0.0f)); }
DEV f32x4 mfma(u32x4 a, u32x4 b, f32x4 c) {
    return __builtin_amdgcn_mfma_f32_16x16x32_bf16(__builtin_bit_cast(bf16x8, a), __builtin_bit_cast(bf16x8, b), c, 0, 0, 0);
}
DEV u32x4 lds_r128(lds_ptr p) { return *(const LAS u32x4*)p; }
DEV f32x4 lds_rf128(lds_ptr p) { return *(const LAS f32x4*)p; }
DEV u32x2 lds_r64(lds_ptr p) { return *(const LAS u32x2*)p; }
DEV uint32_t lds_r32(lds_ptr p) { return *(const LAS uint32_t*)p; }
DEV float lds_rf(lds_ptr p) { return *(const LAS float*)p; }
DEV uint32_t lds_r16(lds_ptr p) { return (uint32_t) * (const LAS uint16_t*)p; }
DEV void lds_w128(lds_ptr p, u32x4 v) { *(LAS u32x4*)p = v; }
DEV void lds_w32(lds_ptr p, uint32_t v) { *(LAS uint32_t*)p = v; }
DEV void lds_w16(lds_ptr p, uint32_t v) { *(LAS uint16_t*)p = (uint16_t)v; }
DEV u32x2 lds_tr(lds_ptr p) {
    return __builtin_bit_cast(u32x2, __builtin_amdgcn_ds_read_tr16_b64_v4i16((LAS s16x4*)p));
}
DEV float bflo(uint32_t v) { return __builtin_bit_cast(float, v << 16); }
DEV float bfhi(uint32_t v) { return __builtin_bit_cast(float, v & 0xffff0000u); }
DEV float ex2(float v) { return __builtin_amdgcn_exp2f(v); }

DEV void dma16nt(const void* sbase, uint32_t voff, uint32_t lds_dst) {
    unsigned keep;
    asm volatile("s_mov_b32 %0, m0\n\ts_mov_b32 m0, %3\n\ts_nop 0\n\tglobal_load_lds_dwordx4 %1, %2 nt\n\ts_mov_b32 m0, %0"
                 : "=&s"(keep) : "v"(voff), "s"(sbase), "s"(lds_dst) : "memory");
}
DEV void dma144(const void* sbase, uint32_t voff, uint32_t lds_dst) {
    unsigned keep;
    asm volatile("s_mov_b32 %0, m0\n\ts_mov_b32 m0, %3\n\ts_nop 0\n\t"
                 "global_load_lds_dwordx4 %1, %2\n\t"
                 "global_load_lds_dwordx4 %1, %2 offset:1024\n\t"
                 "s_mov_b64 exec, 0xffff\n\t"
                 "global_load_lds_dwordx4 %1, %2 offset:2048\n\t"
                 "s_mov_b64 exec, -1\n\ts_mov_b32 m0, %0"
                 : "=&s"(keep) : "v"(voff), "s"(sbase), "s"(lds_dst) : "memory");
}
DEV void dma144nt(const void* sbase, uint32_t v0, uint32_t v1, uint32_t v2, uint32_t lds_dst) {
    unsigned keep;
    asm volatile("s_mov_b32 %0, m0\n\ts_mov_b32 m0, %5\n\ts_nop 0\n\t"
                 "global_load_lds_dwordx4 %1, %4 nt\n\t"
                 "global_load_lds_dwordx4 %2, %4 offset:1024 nt\n\t"
                 "s_mov_b64 exec, 0xffff\n\t"
                 "global_load_lds_dwordx4 %3, %4 offset:2048 nt\n\t"
                 "s_mov_b64 exec, -1\n\ts_mov_b32 m0, %0"
                 : "=&s"(keep) : "v"(v0), "v"(v1), "v"(v2), "s"(sbase), "s"(lds_dst) : "memory");
}
DEV u32x4 gload128(const void* sbase, uint32_t voff) {
    u32x4 v;
    asm volatile("global_load_dwordx4 %0, %1, %2" : "=v"(v) : "v"(voff), "s"(sbase) : "memory");
    return v;
}
DEV uint32_t gload32(const void* sbase, uint32_t voff) {
    uint32_t v;
    asm volatile("global_load_dword %0, %1, %2" : "=v"(v) : "v"(voff), "s"(sbase) : "memory");
    return v;
}
DEV void gstore128(void* sbase, uint32_t voff, u32x4 v) {
    asm volatile("global_store_dwordx4 %0, %1, %2 sc0 sc1\n\ts_nop 1" :: "v"(voff), "v"(v), "s"(sbase) : "memory");
}
#define BARRIER() do { asm volatile("s_waitcnt lgkmcnt(0)" ::: "memory"); __builtin_amdgcn_s_barrier(); asm volatile("" ::: "memory"); } while (0)
#define WAIT_VM(n) asm volatile("s_waitcnt vmcnt(" #n ")" ::: "memory")

DEV void arrive(uint32_t cnt_addr, uint32_t inc) {
    uint32_t t0, t1;
    asm volatile("v_mov_b32 %0, %2\n\tv_mov_b32 %1, %3\n\ts_mov_b64 exec, 1\n\t"
                 "ds_add_u32 %0, %1\n\ts_mov_b64 exec, -1"
                 : "=&v"(t0), "=&v"(t1) : "s"(cnt_addr), "s"(inc) : "memory");
}
DEV uint32_t peek(lds_ptr cnt) { return __hip_atomic_load((LAS uint32_t*)cnt, __ATOMIC_RELAXED, __HIP_MEMORY_SCOPE_WORKGROUP); }
DEV void wait_fields(lds_ptr cnt, uint32_t v, uint32_t mask, uint32_t target) {
    while (((uint32_t)__builtin_amdgcn_readfirstlane(v) & mask) != target) v = peek(cnt);
    asm volatile("" ::: "memory");
}

struct Frags {
    u32x4 Q[3], K[4];
    u32x4 VT[2][2];
};

DEV void pv_norm(const Frags& F, u32x4 P0, u32x4 P1, uint32_t (&o)[3], uint32_t& sumbits) {
    f32x4 zero = {0.f, 0.f, 0.f, 0.f};
    f32x4 oa = mfma(F.VT[0][0], P0, zero);
    f32x4 ob = mfma(F.VT[1][0], P0, zero);
    oa = mfma(F.VT[0][1], P1, oa);
    ob = mfma(F.VT[1][1], P1, ob);
    float sum = ob[2];
    sumbits = __builtin_bit_cast(uint32_t, sum);
    float rs = __builtin_amdgcn_rcpf(sum);
    o[0] = pks(oa[0], oa[1], rs);
    o[1] = pks(oa[2], oa[3], rs);
    o[2] = pks(ob[0], ob[1], rs);
}

struct Sc { f32x4 s0, s1, s2; float s30; };
DEV void scores(const Frags& F, u32x4 Q, const f32x4 (&bias)[3], f32x4 CW, const u32x4 (&mk)[3], u32x4 IHL, Sc& S) {
    S.s0 = mfma(F.K[0], Q, bias[0]);
    S.s1 = mfma(F.K[1], Q, bias[1]);
    S.s2 = mfma(F.K[2], Q, bias[2]);
    f32x4 s3 = mfma(F.K[3], Q, CW);
    S.s30 = s3[0];
    S.s0 = mfma(IHL, mk[0], S.s0);
    S.s1 = mfma(IHL, mk[1], S.s1);
    S.s2 = mfma(IHL, mk[2], S.s2);
}
DEV void exp_pv(const Frags& F, const Sc& S, uint32_t (&o)[3], uint32_t& sumbits) {
    u32x4 P0 = {pk(ex2(S.s0[0]), ex2(S.s0[1])), pk(ex2(S.s0[2]), ex2(S.s0[3])), pk(ex2(S.s1[0]), ex2(S.s1[1])), pk(ex2(S.s1[2]), ex2(S.s1[3]))};
    u32x4 P1 = {pk(ex2(S.s2[0]), ex2(S.s2[1])), pk(ex2(S.s2[2]), ex2(S.s2[3])), pk(ex2(S.s30), 0.f), 0u};
    pv_norm(F, P0, P1, o, sumbits);
}
DEV void scores_safe(const Frags& F, u32x4 Q, const f32x4 (&bias)[3], f32x4 CW, lds_ptr M, Sc& S) {
    const int lane = threadIdx.x & 63;
    const int g = lane >> 4, mm = lane & 15;
    const int v = (mm >> 1) & 7;
    f32x4 zero = {0.f, 0.f, 0.f, 0.f};
    f32x4 s0 = mfma(F.K[0], Q, bias[0]);
    f32x4 s1 = mfma(F.K[1], Q, bias[1]);
    f32x4 s2 = mfma(F.K[2], Q, bias[2]);
    f32x4 s3 = mfma(F.K[3], Q, zero);
    const int h4 = 4 * (__builtin_amdgcn_readfirstlane(threadIdx.x >> 6) & 3);
    const uint32_t bb = lds_r32(M + 128 * mm + 16 * (7 ^ v) + h4);
    const float m48 = bflo(lds_r16(M + 128 * mm + 16 * (6 ^ v) + 14));
    float s30 = __builtin_fmaf(m48, LOG2E, s3[0] + (bflo(bb) + bfhi(bb)));
    u32x2 mv0 = lds_r64(M + 128 * mm + 16 * ((0 + (g >> 1)) ^ v) + 8 * (g & 1));
    u32x2 mv1 = lds_r64(M + 128 * mm + 16 * ((2 + (g >> 1)) ^ v) + 8 * (g & 1));
    u32x2 mv2 = lds_r64(M + 128 * mm + 16 * ((4 + (g >> 1)) ^ v) + 8 * (g & 1));
#define ADDM(s, mv) s[0] = __builtin_fmaf(bflo(mv[0]), LOG2E, s[0]); s[1] = __builtin_fmaf(bfhi(mv[0]), LOG2E, s[1]); \
                    s[2] = __builtin_fmaf(bflo(mv[1]), LOG2E, s[2]); s[3] = __builtin_fmaf(bfhi(mv[1]), LOG2E, s[3]);
    ADDM(s0, mv0) ADDM(s1, mv1) ADDM(s2, mv2)
#undef ADDM
    s30 = (CW[0] == 0.f) ? s30 : -INFINITY;
    float mx = s30;
#pragma unroll
    for (int r = 0; r < 4; ++r) mx = fmaxf(mx, fmaxf(s0[r], fmaxf(s1[r], s2[r])));
    mx = fmaxf(mx, __shfl_xor(mx, 16));
    mx = fmaxf(mx, __shfl_xor(mx, 32));
#pragma unroll
    for (int r = 0; r < 4; ++r) { s0[r] -= mx; s1[r] -= mx; s2[r] -= mx; }
    S.s0 = s0; S.s1 = s1; S.s2 = s2; S.s30 = s30 - mx;
}

template <bool SAFE>
DEV void left_scores(const Frags& F, float cinit, bool valid, u32x4 ql, lds_ptr PLw) {
    const int g = (threadIdx.x & 63) >> 4;
    const f32x4 c = {cinit, 0.f, 0.f, 0.f};
    const f32x4 s0 = mfma(ql, F.K[0], c), s1 = mfma(ql, F.K[1], c), s2 = mfma(ql, F.K[2], c), s3 = mfma(ql, F.K[3], c);
    const float va = (g & 1) ? s1[0] : s0[0], vb = (g & 1) ? s3[0] : s2[0];
    float v = (g & 2) ? vb : va;
    if (SAFE) {
        v = valid ? v : -INFINITY;
        float mx = v;
#pragma unroll
        for (int d = 1; d < 64; d <<= 1) mx = fmaxf(mx, __shfl_xor(mx, d));
        v -= mx;
    }
    lds_w16(PLw, pk(ex2(v), 0.f));
}
struct PLd { u32x2 a0, a1, a2, a3; };
DEV PLd left_ld(lds_ptr PLr) { return PLd{lds_r64(PLr), lds_r64(PLr + 32), lds_r64(PLr + 64), lds_r64(PLr + 96)}; }
DEV void left_pv(const Frags& F, const PLd& a, uint32_t (&o)[3], uint32_t& sumbits) {
    u32x4 P0 = {a.a0[0], a.a0[1], a.a1[0], a.a1[1]}, P1 = {a.a2[0], a.a2[1], a.a3[0], a.a3[1]};
    pv_norm(F, P0, P1, o, sumbits);
}
#define PR0 -1
#define PR1 1
#define PR0B -1
#define PR1B 5
#define PRIO_HOOK(k) do { if (PR1 == k) { if (p == 1) __builtin_amdgcn_s_setprio(1); } if (PR1B == k) { if (p == 1) __builtin_amdgcn_s_setprio(0); } \
    if (PR0 == k) { if (p == 0) __builtin_amdgcn_s_setprio(2); } if (PR0B == k) { if (p == 0) __builtin_amdgcn_s_setprio(0); } } while (0)
#define SCHED_FENCE() __builtin_amdgcn_sched_barrier(0)
#define NPRE0 2

__global__ __launch_bounds__(512, 2) void wattn_kernel(const uint16_t* __restrict__ x, const uint16_t* __restrict__ wqkv,
                                                       const uint16_t* __restrict__ wo, const uint16_t* __restrict__ rpb,
                                                       const int* __restrict__ rpidx, const uint16_t* __restrict__ mask,
                                                       uint16_t* __restrict__ out) {
    extern __shared__ __attribute__((aligned(16))) char smem_[];
    lds_ptr L = (lds_ptr)smem_;
    const uint32_t Lbase = (uint32_t)(uintptr_t)L;
    const int tid = threadIdx.x, lane = tid & 63;
    const int wv = __builtin_amdgcn_readfirstlane(tid >> 6);
    const int h = wv & 3, p = wv >> 2;
    const int g = lane >> 4, m = lane & 15;
    const int blk = blockIdx.x;
    const int wh = blk >> 3, ww0 = (blk & 7) * 4;
    const uint32_t wrapY = (wh == 31) ? (uint32_t)-9633792 : 0u;
    const uint32_t org0 = (uint32_t)(((7 * wh + 3) * 224 + 7 * ww0 + 3) * 192);
    const int role = (h + 2 * p) & 3;

    const uint32_t rsrc = 4 * (tid < 338 ? tid : 337);
    const uint32_t l48 = lane < 48 ? lane : 48;
    const uint32_t ilo = 196u * m + 16u * g;
    const int fq = (wv * 11) >> 5;
    const uint32_t isrcA = ilo + (uint32_t)(3136 * fq + 64 * (wv - 3 * fq));
    const uint32_t isrcB = (wv == 4) ? ilo + 6400u : (wv == 5) ? 196u * l48 + 192u : 9408u + 4u * l48;
    uint32_t xr[3];
#pragma unroll
    for (int j = 0; j < 3; ++j) {
        const uint32_t pos = 144u * h + 64u * j + lane;
        const uint32_t a = (pos * 781u) >> 16, p96 = (pos * 683u) >> 16;
        xr[j] = ((pos << 4) ^ ((p96 & 1u) << 5)) + 41664u * a + (pos >= 336u ? wrapY : 0u) + (org0 + 1344u * p + 8192u - 1024u * j);
    }
    const uint32_t wvoff = (16u * lane) ^ (uint32_t)(32 * ((wv >> 1) & 1));
    uint32_t msrc[2];
    bool mok[2];
#pragma unroll
    for (int j = 0; j < 2; ++j) {
        const int pos = 98 * h + 64 * j + lane;
        const int q = pos >> 3, mk = (pos & 7) ^ ((q >> 1) & 7);
        msrc[j] = 98 * q + (mk < 6 ? 16 * mk : 82);
        mok[j] = (mk < 7) && (j == 0 || lane < 34);
    }
    if (wv == 7 && lane == 0) lds_w32(L + OFF_CNT, 0u);
    asm volatile("" :: "v"(rsrc), "v"(isrcA), "v"(isrcB), "v"(xr[0]), "v"(xr[1]), "v"(xr[2]), "v"(wvoff), "v"(msrc[0]), "v"(msrc[1]));
    const float onec = ((m & 3) >= 2) ? 1.0f : 0.0f;
    const f32x4 ONES = {onec, onec, onec, onec};
    const f32x4 ZERO = {0.f, 0.f, 0.f, 0.f};
    const float qsc = (g & 1) ? 1.0f : QSCALE;
    lds_ptr QL = L + OFF_QL + wv * 256;
    lds_ptr CNT = L + OFF_CNT;
    const uint32_t cnt_addr = Lbase + OFF_CNT;
    lds_ptr wab, wc, wv1;
    {
        const int q4 = m >> 2, p4 = m & 3, sx = 32 * (g & 1);
        lds_ptr rb = L + OFF_W + 576 * (8 * g + q4);
        wab = rb + ((48 * h + 8 * p4) ^ sx);
        wc = rb + ((48 * h + 32 + 8 * (p4 >> 1)) ^ sx) + 192 * (p4 & 1);
        wv1 = rb + 384 + ((48 * h + 32 + 8 * (p4 & 1)) ^ sx);
    }
    const int bperm = 4 * (16 * g + 2 * (m >> 2) + (m & 1));
    const uint32_t bmask = ((m & 3) < 2) ? 0xffffffffu : 0u;
    u32x4 IHL;
    {
        const uint32_t val = (g < 2) ? 0x3FB9u : 0xBB2Cu;
        const int j = m - 8 * (g & 1);
#pragma unroll
        for (int r = 0; r < 4; ++r) IHL[r] = (j == 2 * r) ? val : (j == 2 * r + 1) ? (val << 16) : 0u;
    }
    lds_ptr q3a, mLa, qa, osw, oLw, xa, woa0, woa2, PLw, PLr;
    uint32_t ma0;
    {
        const int vsw = (m >> 1) & 7;
        lds_ptr M = L + OFF_MASK + p * MS;
        ma0 = Lbase + OFF_MASK + p * MS + 128 * m + 16 * ((g & 1) ^ vsw);
        q3a = M + 128 * m + (g < 2 ? 16 * (6 ^ vsw) + 12 : 16 * (7 ^ vsw) + 4 * h);
        mLa = M + 128 * 48 + (lane < 48 ? 2 * lane : 110);
        qa = QL + 64 * p + 16 * g;
        osw = L + OFF_OB + p * OBS + lane * 16 + 4 * (h ^ (g & 1));
        oLw = L + OFF_OL + p * 3072 + lane * 16 + 4 * (h ^ (g & 1));
        xa = L + OFF_X + p * XS + 192 * m + ((16 * g) ^ (((m >> 3) & 1) << 5));
        const int q4 = m >> 2, p4 = m & 3;
        const int rb = 24 * ((q4 >> 1) ^ (g & 1)) + (q4 & 1);
        const int cb = (role < 3) ? 64 * role : 0;
        woa0 = L + OFF_WO + (rb + 4 * g) * 192 + 16 * p4 + cb;
        woa2 = L + OFF_WO + (rb + 16 + 2 * g) * 192 + 16 * p4 + cb;
        PLw = L + OFF_PL + wv * 128 + 2 * lane;
        PLr = L + OFF_PL + wv * 128 + 8 * g;
    }
    uint32_t so[3];
#pragma unroll
    for (int qt = 0; qt < 3; ++qt) {
        const uint32_t t = 16u * qt + m, a = (t * 37u) >> 8;
        so[qt] = 192u * t + 41664u * a + (a >= 4u ? wrapY : 0u) + 16u * g + (uint32_t)(64 * role) + (org0 + 1344u * p);
    }
    asm volatile("" : "+v"(so[0]), "+v"(so[1]), "+v"(so[2]), "+v"(IHL), "+v"(ma0), "+v"(q3a), "+v"(mLa));
    asm volatile("" : "+v"(qa), "+v"(osw), "+v"(xa), "+v"(woa0), "+v"(woa2));
    asm volatile("" : "+v"(wab), "+v"(wc), "+v"(wv1));

    const char* xm = (const char*)x - 8192;
    auto load_w = [&](int s) {
        dma144((const char*)wqkv + 18432 * s + 2304 * wv, wvoff, Lbase + OFF_W + 18432 * s + 2304 * wv);
    };
    auto load_mask = [&](int i) {
        const char* mb = (const char*)mask + (size_t)(4 * blk + i) * 4802;
        const uint32_t dst = Lbase + OFF_MASK + i * MS + 98 * 16 * h;
        if (mok[0]) dma16nt(mb, msrc[0], dst);
        if (mok[1]) dma16nt(mb, msrc[1], dst + 1024);
    };
    u32x4 ivA, ivB = {0u, 0u, 0u, 0u};
    if (wv == 4) ivB = gload128(rpidx, isrcB);
    else if (wv == 5 || wv == 6) ivB[0] = gload32(rpidx, isrcB);
    uint32_t rpbv = gload32(rpb, rsrc);
    ivA = gload128(rpidx, isrcA);
    {
        int y = 7 * wh + 9; if (y >= 224) y -= 224;
        int xx = 7 * (ww0 + (wv >> 1)) + 9; if (xx >= 224) xx -= 224;
        if (lane < 6) dma16nt(x, (uint32_t)((y * 224 + xx) * 192 + 96 * (wv & 1) + 16 * lane), Lbase + OFF_XL + 96 * wv);
    }
    dma144nt(xm, xr[0], xr[1], xr[2], Lbase + OFF_X + p * XS + 2304 * h);
    load_w(0);
    load_w(1);
    load_w(2);

    asm volatile("s_waitcnt vmcnt(14)" : "+v"(rpbv), "+v"(ivB) :: "memory");
    if (tid < 338) *(LAS f32x2*)(L + OFF_RPB + 8 * tid) = f32x2{bflo(rpbv) * LOG2E, bfhi(rpbv) * LOG2E};
    BARRIER();
    auto bias_unit = [&](int f, u32x4 iv) {
        lds_ptr T = L + OFF_RPB;
        const u32x4 b0 = lds_r128(T + 16 * iv[0]), b1 = lds_r128(T + 16 * iv[1]), b2 = lds_r128(T + 16 * iv[2]), b3 = lds_r128(T + 16 * iv[3]);
        lds_ptr D = L + OFF_BH + 1024 * f + 16 * lane;
#pragma unroll
        for (int hb = 0; hb < 4; ++hb) lds_w128(D + 9216 * hb, u32x4{b0[hb], b1[hb], b2[hb], b3[hb]});
    };
    asm volatile("s_waitcnt vmcnt(13)" : "+v"(ivA) :: "memory");
    bias_unit(wv, ivA);
    if (wv == 4) bias_unit(8, ivB);
    else if (wv == 5) {
        const f32x4 b48 = lds_rf128(L + OFF_RPB + 16 * ivB[0]);
        u32x4 pr;
#pragma unroll
        for (int hb = 0; hb < 4; ++hb) {
            const uint32_t hi = pk(b48[hb], 0.f) & 0xffffu;
            pr[hb] = hi | (pk(b48[hb] - bflo(hi), 0.f) << 16);
        }
        const int a48 = 128 * lane + 16 * (7 ^ ((lane >> 1) & 7));
        if (lane < 48) {
#pragma unroll
            for (int i = 0; i < 4; ++i) lds_w128(L + OFF_MASK + i * MS + a48, pr);
        }
    } else if (wv == 6) lds_w128(L + OFF_BL + 16 * lane, lds_r128(L + OFF_RPB + 16 * ivB[0]));
    WAIT_VM(0);
    BARRIER();
    PRIO_HOOK(0);

    u32x4 Wf[5][3];
    auto load_wf = [&](int s) {
        auto fr = [&](lds_ptr a) -> u32x4 {
            u32x2 lo = lds_tr(a + 18432 * s), hi = lds_tr(a + 18432 * s + 2304);
            return u32x4{lo[0], lo[1], hi[0], hi[1]};
        };
        Wf[0][s] = fr(wab);
        Wf[1][s] = fr(wab + 192);
        Wf[2][s] = fr(wc);
        Wf[3][s] = fr(wab + 384);
        const u32x4 t = fr(wv1);
#pragma unroll
        for (int r = 0; r < 4; ++r) Wf[4][s][r] = (uint32_t)__builtin_amdgcn_ds_bpermute(bperm, (int)t[r]) & bmask;
    };
    struct Acc { f32x4 a0, a1, a2, v0, v1; };
    auto proj_step = [&](Acc& A, u32x4 xf, int s, bool first) {
        A.a0 = mfma(Wf[0][s], xf, first ? ZERO : A.a0);
        A.a1 = mfma(Wf[1][s], xf, first ? ZERO : A.a1);
        A.a2 = mfma(Wf[2][s], xf, first ? ZERO : A.a2);
        A.v0 = mfma(xf, Wf[3][s], first ? ZERO : A.v0);
        A.v1 = mfma(xf, Wf[4][s], first ? ONES : A.v1);
    };
    auto proj_fin = [&](const Acc& A, u32x4& Q, u32x4& K, u32x2& V0, u32x2& V1) {
        const uint32_t c0 = pks(A.a2[0], A.a2[1], qsc), c1 = pks(A.a2[2], A.a2[3], qsc);
        const auto sw = __builtin_amdgcn_permlane16_swap(c0, c1, false, false);
        Q[0] = pks(A.a0[0], A.a0[1], QSCALE); Q[1] = pks(A.a0[2], A.a0[3], QSCALE); Q[2] = sw[0];
        K[0] = pk(A.a1[0], A.a1[1]); K[1] = pk(A.a1[2], A.a1[3]); K[2] = sw[1];
        V0 = u32x2{pk(A.v0[0], A.v0[1]), pk(A.v0[2], A.v0[3])};
        V1 = u32x2{pk(A.v1[0], A.v1[1]), pk(A.v1[2], A.v1[3])};
    };
    Frags F;
    F.Q[0][3] = F.Q[1][3] = F.Q[2][3] = 0u;
    F.K[0][3] = F.K[1][3] = F.K[2][3] = 0u;
    F.K[3][3] = (g == 0) ? 0x3FB90000u : (g == 1) ? 0xBB2C0000u : (g == 2) ? 0x3F803F80u : 0u;
    u32x2 V0L, V1L;
    f32x4 bias[3][3];
    float biasL;
    u32x2 V0[3], V1[3];
    auto set_vt = [&]() {
        F.VT[0][0] = u32x4{V0[0][0], V0[0][1], V0[1][0], V0[1][1]};
        F.VT[0][1] = u32x4{V0[2][0], V0[2][1], V0L[0], V0L[1]};
        F.VT[1][0] = u32x4{V1[0][0], V1[0][1], V1[1][0], V1[1][1]};
        F.VT[1][1] = u32x4{V1[2][0], V1[2][1], V1L[0], V1L[1]};
    };
    auto proj_tile = [&](lds_ptr a, u32x4& Q, u32x4& K, u32x2& v0, u32x2& v1) {
        u32x4 xf0 = lds_r128(a), xf1 = lds_r128(a + 64), xf2 = lds_r128(a + 128);
        Acc A;
        proj_step(A, xf0, 0, true); proj_step(A, xf1, 1, false); proj_step(A, xf2, 2, false);
        proj_fin(A, Q, K, v0, v1);
    };

    {
        lds_ptr xl = L + OFF_XL + 192 * (m >> 2) + 16 * g;
        const u32x4 xl0 = lds_r128(xl), xl1 = lds_r128(xl + 64), xl2 = lds_r128(xl + 128);
        load_wf(0); load_wf(1); load_wf(2);
        const u32x4 x00 = lds_r128(xa), x01 = lds_r128(xa + 64), x02 = lds_r128(xa + 128);
        {
            lds_ptr B = L + OFF_BH + 9216 * h + 16 * lane;
#pragma unroll
            for (int qt = 0; qt < 3; ++qt)
#pragma unroll
                for (int kt = 0; kt < 3; ++kt) bias[qt][kt] = lds_rf128(B + 1024 * (3 * qt + kt));
            biasL = lds_rf(L + OFF_BL + 16 * lane + 4 * h);
        }
        load_mask(p);
        {
            uint32_t v0 = xr[0], v1 = xr[1], v2 = xr[2];
            if (p == 1 && ww0 == 28) {
    #pragma unroll
                for (int j = 0; j < 3; ++j) {
                    const uint32_t pos = 144u * h + 64u * j + lane;
                    const uint32_t a = (pos * 781u) >> 16;
                    const uint32_t d = (pos - 84u * a >= 48u) ? (uint32_t)-43008 : 0u;
                    if (j == 0) v0 += d; else if (j == 1) v1 += d; else v2 += d;
                }
            }
            dma144nt(xm + 2688, v0, v1, v2, Lbase + OFF_X + (p + 2) * XS + 2304 * h);
        }
        load_mask(p + 2);
        Acc A, B;
        proj_step(A, xl0, 0, true); proj_step(A, xl1, 1, false); proj_step(A, xl2, 2, false);
        arrive(cnt_addr, 1u);
        proj_step(B, x00, 0, true); proj_step(B, x01, 1, false); proj_step(B, x02, 2, false);
        u32x4 QLr = {0u, 0u, 0u, 0u};
        proj_fin(A, QLr, F.K[3], V0L, V1L);
        if ((m & 3) == 0) lds_w128(QL + (m >> 2) * 64 + g * 16, QLr);
        proj_fin(B, F.Q[0], F.K[0], V0[0], V1[0]);
        proj_tile(xa + 3072, F.Q[1], F.K[1], V0[1], V1[1]);
        proj_tile(xa + 6144, F.Q[2], F.K[2], V0[2], V1[2]);
        set_vt();
    }
    WAIT_VM(2);
    arrive(cnt_addr, 16u << (4 * p));
    PRIO_HOOK(1);
    wait_fields(CNT, peek(CNT), 0xFu | (0xF0u << (4 * p)), 8u | (0x40u << (4 * p)));

    auto wo_frag = [&](int colb, int s) -> u32x4 {
        lds_ptr a = (s == 2 ? woa2 : woa0 + (s == 1 ? 384 : 0)) + colb;
        u32x2 lo = lds_tr(a), hi = lds_tr(a + 48 * 192);
        return u32x4{lo[0], lo[1], hi[0], hi[1]};
    };
    struct Pre { u32x4 ql, mk[3][3]; uint32_t q3[3], mL; };
    auto fetch = [&](auto itc, Pre& P) {
        constexpr int it = decltype(itc)::value;
        constexpr int MO = 2 * MS * it;
        P.ql = lds_r128(qa + 128 * it);
        P.mL = lds_r16(mLa + MO);
        uint32_t a0 = ma0;
        asm volatile("" : "+v"(a0));
        lds_ptr ma0p = (lds_ptr)(uintptr_t)a0, ma1p = (lds_ptr)(uintptr_t)(a0 ^ 32u), ma2p = (lds_ptr)(uintptr_t)(a0 ^ 64u);
#pragma unroll
        for (int tt = 0; tt < 3; ++tt) P.q3[tt] = lds_r32(q3a + MO + 2048 * tt);
#pragma unroll
        for (int tt = 0; tt < (it == 0 ? NPRE0 : 3); ++tt) {
            P.mk[tt][0] = lds_r128(ma0p + MO + 2048 * tt);
            P.mk[tt][1] = lds_r128(ma1p + MO + 2048 * tt);
            P.mk[tt][2] = lds_r128(ma2p + MO + 2048 * tt);
        }
    };
    auto window = [&](auto itc, Pre& P) {
        constexpr int it = decltype(itc)::value;
        constexpr int OO = 2 * OBS * it;
        const int i = p + 2 * it;
        uint32_t o[4][3];
        const f32x4 CW = {(g == i) ? 0.f : NEG_BIG, 0.f, 0.f, 0.f};
        const bool validL = (lane < 48) || (lane == 48 + 4 * i);
        uint32_t sb[4];
        asm volatile("" : "+v"(P.mL));
        const float cL = validL ? __builtin_fmaf(bflo(P.mL), LOG2E, biasL) : NEG_BIG;
        auto check = [&]() -> bool {
            const uint32_t lo = min(min(sb[0], sb[1]), min(sb[2], sb[3])), hi = max(max(sb[0], sb[1]), max(sb[2], sb[3]));
            const bool bad = (lo < 0x2B800000u) || (hi > 0x53800000u);
            return __builtin_amdgcn_ballot_w64(bad) != 0ull;
        };
        {
#pragma unroll
            for (int tt = 0; tt < 3; ++tt) F.Q[tt][3] = P.q3[tt];
            Sc S0, S1, S2;
            left_scores<false>(F, cL, validL, P.ql, PLw);
            const PLd pl = left_ld(PLr);
            scores(F, F.Q[0], bias[0], CW, P.mk[0], IHL, S0);
            if (it == 0) {
                uint32_t a0 = ma0;
                asm volatile("" : "+v"(a0));
                lds_ptr ma0p = (lds_ptr)(uintptr_t)a0, ma1p = (lds_ptr)(uintptr_t)(a0 ^ 32u), ma2p = (lds_ptr)(uintptr_t)(a0 ^ 64u);
#pragma unroll
                for (int tt = NPRE0; tt < 3; ++tt) {
                    P.mk[tt][0] = lds_r128(ma0p + 2048 * tt);
                    P.mk[tt][1] = lds_r128(ma1p + 2048 * tt);
                    P.mk[tt][2] = lds_r128(ma2p + 2048 * tt);
                }
            }
            SCHED_FENCE();
            scores(F, F.Q[1], bias[1], CW, P.mk[1], IHL, S1);
            exp_pv(F, S0, o[0], sb[0]);
            scores(F, F.Q[2], bias[2], CW, P.mk[2], IHL, S2);
            exp_pv(F, S1, o[1], sb[1]);
            left_pv(F, pl, o[3], sb[3]);
            exp_pv(F, S2, o[2], sb[2]);
        }
        if (__builtin_expect(check(), 0)) {
            lds_ptr M = L + OFF_MASK + i * MS;
#pragma unroll
            for (int qt = 0; qt < 3; ++qt) {
                Sc S;
                u32x4 Q = F.Q[qt];
                Q[3] = 0u;
                scores_safe(F, Q, bias[qt], CW, M + 2048 * qt, S);
                exp_pv(F, S, o[qt], sb[qt]);
            }
            left_scores<true>(F, cL, validL, lds_r128(qa + 128 * it), PLw);
            left_pv(F, left_ld(PLr), o[3], sb[3]);
        }
#pragma unroll
        for (int qt = 0; qt < 3; ++qt)
#pragma unroll
            for (int s = 0; s < 3; ++s) lds_w32(osw + OO + qt * 3072 + s * 1024, o[qt][s]);
        if (m == it) {
#pragma unroll
            for (int s = 0; s < 3; ++s) lds_w32(oLw + s * 1024, o[3][s]);
        }
        if (it == 0) WAIT_VM(0);
        arrive(cnt_addr, (it == 0 ? 0x1000u : 0x100000u) << (4 * p));
    };
    auto out_store = [&](const f32x4& c0, const f32x4& c1, uint32_t off) {
        u32x4 r = {pk(c0[0], c0[1]), pk(c0[2], c0[3]), pk(c1[0], c1[1]), pk(c1[2], c1[3])};
        gstore128(out, off, r);
    };

    Pre P;
    fetch(std::integral_constant<int, 0>{}, P);
    SCHED_FENCE();
    if (p == 0) {
        dma144((const char*)wo + 4608 * h, 16u * lane, Lbase + OFF_WO + 4608 * h);
        dma144((const char*)wo + 4608 * h + 2304, 16u * lane, Lbase + OFF_WO + 4608 * h + 2304);
    }
    window(std::integral_constant<int, 0>{}, P);
    PRIO_HOOK(2);
    proj_tile(xa + 2 * XS, F.Q[0], F.K[0], V0[0], V1[0]);
    proj_tile(xa + 2 * XS + 3072, F.Q[1], F.K[1], V0[1], V1[1]);
    proj_tile(xa + 2 * XS + 6144, F.Q[2], F.K[2], V0[2], V1[2]);
    set_vt();
    PRIO_HOOK(3);
    wait_fields(CNT, peek(CNT), p ? 0xFF000u : 0xF000u, p ? 0x44000u : 0x4000u);
    lds_ptr ord = (lds_ptr)(uintptr_t)((uint32_t)(uintptr_t)osw & ~15u);
    {
        u32x4 of[3][3], w[6];
        if (role < 3) {
#pragma unroll
            for (int s = 0; s < 3; ++s) {
                w[s] = wo_frag(0, s); w[3 + s] = wo_frag(8, s);
#pragma unroll
                for (int qt = 0; qt < 3; ++qt) of[qt][s] = lds_r128(ord + qt * 3072 + s * 1024);
            }
        }
        fetch(std::integral_constant<int, 1>{}, P);
        SCHED_FENCE();
        if (role < 3) {
            f32x4 c0[3], c1[3];
#pragma unroll
            for (int s = 0; s < 3; ++s)
#pragma unroll
                for (int qt = 0; qt < 3; ++qt) {
                    c0[qt] = mfma(w[s], of[qt][s], s ? c0[qt] : ZERO);
                    c1[qt] = mfma(w[3 + s], of[qt][s], s ? c1[qt] : ZERO);
                }
#pragma unroll
            for (int qt = 0; qt < 3; ++qt) out_store(c0[qt], c1[qt], so[qt]);
        }
    }
    PRIO_HOOK(4);
    window(std::integral_constant<int, 1>{}, P);
    PRIO_HOOK(5);
    if (role < 3) {
        uint32_t sx[3];
#pragma unroll
        for (int qt = 0; qt < 3; ++qt) sx[qt] = so[qt] + 2688u;
        if (ww0 + p + 2 == 31) {
            uint32_t mo = lane;
            asm volatile("" : "+v"(mo));
            mo &= 15u;
#pragma unroll
            for (int qt = 0; qt < 3; ++qt) {
                const uint32_t t = 16u * qt + mo, a = (t * 37u) >> 8;
                sx[qt] += (t - 7u * a >= 4u) ? (uint32_t)-43008 : 0u;
            }
        }
        u32x4 w[6];
#pragma unroll
        for (int s = 0; s < 3; ++s) { w[s] = wo_frag(0, s); w[3 + s] = wo_frag(8, s); }
        wait_fields(CNT, peek(CNT), 0xF00000u << (4 * p), 0x400000u << (4 * p));
        u32x4 of[3][3];
#pragma unroll
        for (int s = 0; s < 3; ++s)
#pragma unroll
            for (int qt = 0; qt < 3; ++qt) of[qt][s] = lds_r128(ord + 2 * OBS + qt * 3072 + s * 1024);
        f32x4 c0[3], c1[3];
#pragma unroll
        for (int s = 0; s < 3; ++s)
#pragma unroll
            for (int qt = 0; qt < 3; ++qt) {
                c0[qt] = mfma(w[s], of[qt][s], s ? c0[qt] : ZERO);
                c1[qt] = mfma(w[3 + s], of[qt][s], s ? c1[qt] : ZERO);
            }
#pragma unroll
        for (int qt = 0; qt < 3; ++qt) out_store(c0[qt], c1[qt], sx[qt]);
    } else {
        u32x4 w[3][6];
#pragma unroll
        for (int i2 = 0; i2 < 3; ++i2)
#pragma unroll
            for (int s = 0; s < 3; ++s) { w[i2][s] = wo_frag(64 * i2, s); w[i2][3 + s] = wo_frag(64 * i2 + 8, s); }
        int y = 7 * wh + 9; if (y >= 224) y -= 224;
        int xx = 7 * (ww0 + p + 2 * (m & 1)) + 9; if (xx >= 224) xx -= 224;
        const uint32_t dst = (uint32_t)((y * 224 + xx) * 192 + 16 * g);
        wait_fields(CNT, peek(CNT), 0xF00000u << (4 * p), 0x400000u << (4 * p));
        lds_ptr orr = L + OFF_OL + p * 3072 + lane * 16;
        u32x4 of0 = lds_r128(orr), of1 = lds_r128(orr + 1024), of2 = lds_r128(orr + 2048);
        f32x4 c0[3], c1[3];
#pragma unroll
        for (int i2 = 0; i2 < 3; ++i2) { c0[i2] = mfma(w[i2][0], of0, ZERO); c1[i2] = mfma(w[i2][3], of0, ZERO); }
#pragma unroll
        for (int i2 = 0; i2 < 3; ++i2) { c0[i2] = mfma(w[i2][1], of1, c0[i2]); c1[i2] = mfma(w[i2][4], of1, c1[i2]); }
#pragma unroll
        for (int i2 = 0; i2 < 3; ++i2) { c0[i2] = mfma(w[i2][2], of2, c0[i2]); c1[i2] = mfma(w[i2][5], of2, c1[i2]); }
        if (m < 2) {
#pragma unroll
            for (int i2 = 0; i2 < 3; ++i2) out_store(c0[i2], c1[i2], dst + 64 * i2);
        }
    }
}

extern "C" void kernel_launch(void* const* d_in, const int* in_sizes, int n_in, void* d_out, int out_size, void* d_ws,
                              size_t ws_size, hipStream_t stream) {
    (void)in_sizes; (void)n_in; (void)out_size; (void)d_ws; (void)ws_size;
    (void)hipFuncSetAttribute((const void*)wattn_kernel, hipFuncAttributeMaxDynamicSharedMemorySize, LDS_TOTAL);
    wattn_kernel<<<256, 512, LDS_TOTAL, stream>>>((const uint16_t*)d_in[0], (const uint16_t*)d_in[1], (const uint16_t*)d_in[2],
                                                  (const uint16_t*)d_in[3], (const int*)d_in[4], (const uint16_t*)d_in[5],
                                                  (uint16_t*)d_out);
}
```

```cpp
#include <hip/hip_runtime.h>
#include <hip/hip_bf16.h>
#include <stdint.h>
#include <type_traits>

typedef __attribute__((ext_vector_type(8))) __bf16 bf16x8;
typedef __attribute__((ext_vector_type(2))) __bf16 bf16x2;
typedef __attribute__((ext_vector_type(4))) float f32x4;
typedef __attribute__((ext_vector_type(2))) float f32x2;
typedef __attribute__((ext_vector_type(4))) uint32_t u32x4;
typedef __attribute__((ext_vector_type(2))) uint32_t u32x2;
typedef __attribute__((ext_vector_type(4))) short s16x4;

#define DEV __device__ __forceinline__
#define LAS __attribute__((address_space(3)))
typedef LAS char* lds_ptr;

constexpr int OFF_W = 0;
constexpr int OFF_OB = 0, OBS = 9216;
constexpr int OFF_OL = 36864;
constexpr int OFF_RPB = 55296;
constexpr int OFF_X = 58016, XS = 9216;
constexpr int OFF_XL = OFF_X + 4 * XS;
constexpr int OFF_MASK = (OFF_XL + 768 + 127) / 128 * 128, MS = 6272;
constexpr int OFF_WO = OFF_MASK + 4 * MS;
constexpr int OFF_BH = OFF_WO;
constexpr int OFF_QL = OFF_BH + 36864;
constexpr int OFF_PL = OFF_QL + 2048;
constexpr int OFF_BL = OFF_PL + 1024;
constexpr int OFF_CNT = OFF_BL + 1024;
constexpr int LDS_TOTAL = OFF_CNT + 64;

constexpr float LOG2E = 1.4426950408889634f;
constexpr float QSCALE = 0.20412414523193154f * 1.4426950408889634f;
constexpr float NEG_BIG = -1.0e30f;

DEV uint32_t pk(float a, float b) {
    f32x2 v = {a, b};
    bf16x2 r = __builtin_convertvector(v, bf16x2);
    return __builtin_bit_cast(uint32_t, r);
}
DEV uint32_t pks(float a, float b, float s) { return pk(a * s, __builtin_fmaf(b, s, 0.0f)); }
DEV f32x4 mfma(u32x4 a, u32x4 b, f32x4 c) {
    return __builtin_amdgcn_mfma_f32_16x16x32_bf16(__builtin_bit_cast(bf16x8, a), __builtin_bit_cast(bf16x8, b), c, 0, 0, 0);
}
DEV u32x4 lds_r128(lds_ptr p) { return *(const LAS u32x4*)p; }
DEV f32x4 lds_rf128(lds_ptr p) { return *(const LAS f32x4*)p; }
DEV u32x2 lds_r64(lds_ptr p) { return *(const LAS u32x2*)p; }
DEV uint32_t lds_r32(lds_ptr p) { return *(const LAS uint32_t*)p; }
DEV float lds_rf(lds_ptr p) { return *(const LAS float*)p; }
DEV uint32_t lds_r16(lds_ptr p) { return (uint32_t) * (const LAS uint16_t*)p; }
DEV void lds_w128(lds_ptr p, u32x4 v) { *(LAS u32x4*)p = v; }
DEV void lds_w32(lds_ptr p, uint32_t v) { *(LAS uint32_t*)p = v; }
DEV void lds_w16(lds_ptr p, uint32_t v) { *(LAS uint16_t*)p = (uint16_t)v; }
DEV u32x2 lds_tr(lds_ptr p) {
    return __builtin_bit_cast(u32x2, __builtin_amdgcn_ds_read_tr16_b64_v4i16((LAS s16x4*)p));
}
DEV float bflo(uint32_t v) { return __builtin_bit_cast(float, v << 16); }
DEV float bfhi(uint32_t v) { return __builtin_bit_cast(float, v & 0xffff0000u); }
DEV float ex2(float v) { return __builtin_amdgcn_exp2f(v); }

DEV void dma16nt(const void* sbase, uint32_t voff, uint32_t lds_dst) {
    unsigned keep;
    asm volatile("s_mov_b32 %0, m0\n\ts_mov_b32 m0, %3\n\ts_nop 0\n\tglobal_load_lds_dwordx4 %1, %2 nt\n\ts_mov_b32 m0, %0"
                 : "=&s"(keep) : "v"(voff), "s"(sbase), "s"(lds_dst) : "memory");
}
DEV void dma144(const void* sbase, uint32_t voff, uint32_t lds_dst) {
    unsigned keep;
    asm volatile("s_mov_b32 %0, m0\n\ts_mov_b32 m0, %3\n\ts_nop 0\n\t"
                 "global_load_lds_dwordx4 %1, %2\n\t"
                 "global_load_lds_dwordx4 %1, %2 offset:1024\n\t"
                 "s_mov_b64 exec, 0xffff\n\t"
                 "global_load_lds_dwordx4 %1, %2 offset:2048\n\t"
                 "s_mov_b64 exec, -1\n\ts_mov_b32 m0, %0"
                 : "=&s"(keep) : "v"(voff), "s"(sbase), "s"(lds_dst) : "memory");
}
DEV void dma144nt(const void* sbase, uint32_t v0, uint32_t v1, uint32_t v2, uint32_t lds_dst) {
    unsigned keep;
    asm volatile("s_mov_b32 %0, m0\n\ts_mov_b32 m0, %5\n\ts_nop 0\n\t"
                 "global_load_lds_dwordx4 %1, %4 nt\n\t"
                 "global_load_lds_dwordx4 %2, %4 offset:1024 nt\n\t"
                 "s_mov_b64 exec, 0xffff\n\t"
                 "global_load_lds_dwordx4 %3, %4 offset:2048 nt\n\t"
                 "s_mov_b64 exec, -1\n\ts_mov_b32 m0, %0"
                 : "=&s"(keep) : "v"(v0), "v"(v1), "v"(v2), "s"(sbase), "s"(lds_dst) : "memory");
}
DEV void dma4(const void* sbase, uint32_t v0, uint32_t v1, uint32_t lds_dst) {
    unsigned keep;
    asm volatile("s_mov_b32 %0, m0\n\ts_mov_b32 m0, %4\n\ts_nop 0\n\t"
                 "global_load_lds_dwordx4 %1, %3\n\t"
                 "global_load_lds_dwordx4 %2, %3 offset:1024\n\t"
                 "global_load_lds_dwordx4 %2, %3 offset:2048\n\t"
                 "global_load_lds_dwordx4 %2, %3 offset:3072\n\t"
                 "s_mov_b32 m0, %0"
                 : "=&s"(keep) : "v"(v0), "v"(v1), "s"(sbase), "s"(lds_dst) : "memory");
}
DEV void dma96(const void* sbase, uint32_t v0, uint32_t lds_dst) {
    unsigned keep;
    asm volatile("s_mov_b32 %0, m0\n\ts_mov_b32 m0, %3\n\ts_nop 0\n\t"
                 "global_load_lds_dwordx4 %1, %2\n\t"
                 "s_mov_b32 exec_hi, 0\n\t"
                 "global_load_lds_dwordx4 %1, %2 offset:1024\n\t"
                 "s_mov_b32 exec_hi, -1\n\ts_mov_b32 m0, %0"
                 : "=&s"(keep) : "v"(v0), "s"(sbase), "s"(lds_dst) : "memory");
}
DEV void gstore128(void* sbase, uint32_t voff, u32x4 v) {
    asm volatile("global_store_dwordx4 %0, %1, %2 sc0 sc1\n\ts_nop 1" :: "v"(voff), "v"(v), "s"(sbase) : "memory");
}
#define BARRIER() do { asm volatile("s_waitcnt lgkmcnt(0)" ::: "memory"); __builtin_amdgcn_s_barrier(); asm volatile("" ::: "memory"); } while (0)
#define WAIT_VM(n) asm volatile("s_waitcnt vmcnt(" #n ")" ::: "memory")

DEV void arrive(uint32_t cnt_addr, uint32_t inc) {
    uint32_t t0, t1;
    asm volatile("v_mov_b32 %0, %2\n\tv_mov_b32 %1, %3\n\ts_mov_b64 exec, 1\n\t"
                 "ds_add_u32 %0, %1\n\ts_mov_b64 exec, -1"
                 : "=&v"(t0), "=&v"(t1) : "s"(cnt_addr), "s"(inc) : "memory");
}
DEV uint32_t peek(lds_ptr cnt) { return __hip_atomic_load((LAS uint32_t*)cnt, __ATOMIC_RELAXED, __HIP_MEMORY_SCOPE_WORKGROUP); }
DEV void wait_fields(lds_ptr cnt, uint32_t v, uint32_t mask, uint32_t target) {
    while (((uint32_t)__builtin_amdgcn_readfirstlane(v) & mask) != target) v = peek(cnt);
    asm volatile("" ::: "memory");
}

struct Frags {
    u32x4 Q[3], K[4];
    u32x4 VT[2][2];
};

DEV void pv_norm(const Frags& F, u32x4 P0, u32x4 P1, uint32_t (&o)[3], uint32_t& sumbits) {
    f32x4 zero = {0.f, 0.f, 0.f, 0.f};
    f32x4 oa = mfma(F.VT[0][0], P0, zero);
    f32x4 ob = mfma(F.VT[1][0], P0, zero);
    oa = mfma(F.VT[0][1], P1, oa);
    ob = mfma(F.VT[1][1], P1, ob);
    float sum = ob[2];
    sumbits = __builtin_bit_cast(uint32_t, sum);
    float rs = __builtin_amdgcn_rcpf(sum);
    o[0] = pks(oa[0], oa[1], rs);
    o[1] = pks(oa[2], oa[3], rs);
    o[2] = pks(ob[0], ob[1], rs);
}

struct Sc { f32x4 s0, s1, s2; float s30; };
DEV void scores(const Frags& F, u32x4 Q, const f32x4 (&bias)[3], f32x4 CW, const u32x4 (&mk)[3], u32x4 IHL, Sc& S) {
    S.s0 = mfma(F.K[0], Q, bias[0]);
    S.s1 = mfma(F.K[1], Q, bias[1]);
    S.s2 = mfma(F.K[2], Q, bias[2]);
    f32x4 s3 = mfma(F.K[3], Q, CW);
    S.s30 = s3[0];
    S.s0 = mfma(IHL, mk[0], S.s0);
    S.s1 = mfma(IHL, mk[1], S.s1);
    S.s2 = mfma(IHL, mk[2], S.s2);
}
DEV void exp_pv(const Frags& F, const Sc& S, uint32_t (&o)[3], uint32_t& sumbits) {
    u32x4 P0 = {pk(ex2(S.s0[0]), ex2(S.s0[1])), pk(ex2(S.s0[2]), ex2(S.s0[3])), pk(ex2(S.s1[0]), ex2(S.s1[1])), pk(ex2(S.s1[2]), ex2(S.s1[3]))};
    u32x4 P1 = {pk(ex2(S.s2[0]), ex2(S.s2[1])), pk(ex2(S.s2[2]), ex2(S.s2[3])), pk(ex2(S.s30), 0.f), 0u};
    pv_norm(F, P0, P1, o, sumbits);
}
DEV void scores_safe(const Frags& F, u32x4 Q, const f32x4 (&bias)[3], f32x4 CW, lds_ptr M, Sc& S) {
    const int lane = threadIdx.x & 63;
    const int g = lane >> 4, mm = lane & 15;
    const int v = (mm >> 1) & 7;
    f32x4 zero = {0.f, 0.f, 0.f, 0.f};
    f32x4 s0 = mfma(F.K[0], Q, bias[0]);
    f32x4 s1 = mfma(F.K[1], Q, bias[1]);
    f32x4 s2 = mfma(F.K[2], Q, bias[2]);
    f32x4 s3 = mfma(F.K[3], Q, zero);
    const int h4 = 4 * (__builtin_amdgcn_readfirstlane(threadIdx.x >> 6) & 3);
    const uint32_t bb = lds_r32(M + 128 * mm + 16 * (7 ^ v) + h4);
    const float m48 = bflo(lds_r16(M + 128 * mm + 16 * (6 ^ v) + 14));
    float s30 = __builtin_fmaf(m48, LOG2E, s3[0] + (bflo(bb) + bfhi(bb)));
    u32x2 mv0 = lds_r64(M + 128 * mm + 16 * ((0 + (g >> 1)) ^ v) + 8 * (g & 1));
    u32x2 mv1 = lds_r64(M + 128 * mm + 16 * ((2 + (g >> 1)) ^ v) + 8 * (g & 1));
    u32x2 mv2 = lds_r64(M + 128 * mm + 16 * ((4 + (g >> 1)) ^ v) + 8 * (g & 1));
#define ADDM(s, mv) s[0] = __builtin_fmaf(bflo(mv[0]), LOG2E, s[0]); s[1] = __builtin_fmaf(bfhi(mv[0]), LOG2E, s[1]); \
                    s[2] = __builtin_fmaf(bflo(mv[1]), LOG2E, s[2]); s[3] = __builtin_fmaf(bfhi(mv[1]), LOG2E, s[3]);
    ADDM(s0, mv0) ADDM(s1, mv1) ADDM(s2, mv2)
#undef ADDM
    s30 = (CW[0] == 0.f) ? s30 : -INFINITY;
    float mx = s30;
#pragma unroll
    for (int r = 0; r < 4; ++r) mx = fmaxf(mx, fmaxf(s0[r], fmaxf(s1[r], s2[r])));
    mx = fmaxf(mx, __shfl_xor(mx, 16));
    mx = fmaxf(mx, __shfl_xor(mx, 32));
#pragma unroll
    for (int r = 0; r < 4; ++r) { s0[r] -= mx; s1[r] -= mx; s2[r] -= mx; }
    S.s0 = s0; S.s1 = s1; S.s2 = s2; S.s30 = s30 - mx;
}

template <bool SAFE>
DEV void left_scores(const Frags& F, float cinit, bool valid, u32x4 ql, lds_ptr PLw) {
    const int g = (threadIdx.x & 63) >> 4;
    const f32x4 c = {cinit, 0.f, 0.f, 0.f};
    const f32x4 s0 = mfma(ql, F.K[0], c), s1 = mfma(ql, F.K[1], c), s2 = mfma(ql, F.K[2], c), s3 = mfma(ql, F.K[3], c);
    const float va = (g & 1) ? s1[0] : s0[0], vb = (g & 1) ? s3[0] : s2[0];
    float v = (g & 2) ? vb : va;
    if (SAFE) {
        v = valid ? v : -INFINITY;
        float mx = v;
#pragma unroll
        for (int d = 1; d < 64; d <<= 1) mx = fmaxf(mx, __shfl_xor(mx, d));
        v -= mx;
    }
    lds_w16(PLw, pk(ex2(v), 0.f));
}
struct PLd { u32x2 a0, a1, a2, a3; };
DEV PLd left_ld(lds_ptr PLr) { return PLd{lds_r64(PLr), lds_r64(PLr + 32), lds_r64(PLr + 64), lds_r64(PLr + 96)}; }
DEV void left_pv(const Frags& F, const PLd& a, uint32_t (&o)[3], uint32_t& sumbits) {
    u32x4 P0 = {a.a0[0], a.a0[1], a.a1[0], a.a1[1]}, P1 = {a.a2[0], a.a2[1], a.a3[0], a.a3[1]};
    pv_norm(F, P0, P1, o, sumbits);
}
#define PR0 -1
#define PR1 1
#define PR0B -1
#define PR1B 5
#define PRIO_HOOK(k) do { if (PR1 == k) { if (p == 1) __builtin_amdgcn_s_setprio(1); } if (PR1B == k) { if (p == 1) __builtin_amdgcn_s_setprio(0); } \
    if (PR0 == k) { if (p == 0) __builtin_amdgcn_s_setprio(2); } if (PR0B == k) { if (p == 0) __builtin_amdgcn_s_setprio(0); } } while (0)
#define PPRO 2
#define SCHED_FENCE() __builtin_amdgcn_sched_barrier(0)
#define NPRE0 2

__global__ __launch_bounds__(512, 2) void wattn_kernel(const uint16_t* __restrict__ x, const uint16_t* __restrict__ wqkv,
                                                       const uint16_t* __restrict__ wo, const uint16_t* __restrict__ rpb,
                                                       const int* __restrict__ rpidx, const uint16_t* __restrict__ mask,
                                                       uint16_t* __restrict__ out) {
    extern __shared__ __attribute__((aligned(16))) char smem_[];
    lds_ptr L = (lds_ptr)smem_;
    const uint32_t Lbase = (uint32_t)(uintptr_t)L;
    const int tid = threadIdx.x, lane = tid & 63;
    const int wv = __builtin_amdgcn_readfirstlane(tid >> 6);
    const int h = wv & 3, p = wv >> 2;
    const int g = lane >> 4, m = lane & 15;
    const int blk = blockIdx.x;
    const int wh = blk >> 3, ww0 = (blk & 7) * 4;
    const uint32_t wrapY = (wh == 31) ? (uint32_t)-9633792 : 0u;
    const uint32_t org0 = (uint32_t)(((7 * wh + 3) * 224 + 7 * ww0 + 3) * 192);
    const int role = (h + 2 * p) & 3;

    const uint32_t rsrc = 8u * lane, rsrc3 = 8u * (lane < 41 ? lane + 128 : 168);
    const uint32_t l48 = lane < 48 ? lane : 48;
    const uint32_t ilo = 196u * m + 16u * g;
    const uint32_t isrcA = ilo + (uint32_t)(h == 3 ? 3136 : 64 * h);
    const uint32_t isrcB = ilo + (uint32_t)(h < 2 ? 3136 + 64 + 64 * h : 6272 - 128 + 64 * h);
    const uint32_t isrcC = (h == 0) ? ilo + 6400u : (h == 1) ? 196u * l48 + 180u : 9396u + 4u * l48;
    const uint32_t wvA = (16u * lane) ^ (uint32_t)(32 * (h & 1)), wvB = wvA ^ 32u, wvM = lane < 32 ? wvA : wvB;
    if (wv == 7 && lane == 0) lds_w32(L + OFF_CNT, 0u);
    if (PPRO && p == 1) __builtin_amdgcn_s_setprio(PPRO);
    asm volatile("" :: "v"(rsrc), "v"(rsrc3), "v"(isrcA), "v"(isrcB), "v"(isrcC), "v"(wvA), "v"(wvM));
    const char* xm = (const char*)x - 8192;
    u32x4 ivA, ivB, ivC;
    u32x2 rp0, rp1, rp2;
    asm volatile("s_cmp_eq_u32 %11, 0\n\ts_cbranch_scc1 1f\n\t"
                 "global_load_dwordx2 %0, %6, %12\n\t"
                 "global_load_dwordx2 %1, %6, %12 offset:512\n\t"
                 "global_load_dwordx2 %2, %7, %12\n\t"
                 "global_load_dwordx4 %3, %8, %13\n\t"
                 "global_load_dwordx4 %4, %9, %13\n\t"
                 "global_load_dwordx4 %5, %10, %13\n"
                 "1:"
                 : "=&v"(rp0), "=&v"(rp1), "=&v"(rp2), "=&v"(ivA), "=&v"(ivB), "=&v"(ivC)
                 : "v"(rsrc), "v"(rsrc3), "v"(isrcA), "v"(isrcB), "v"(isrcC), "s"(p), "s"(rpb), "s"(rpidx) : "scc", "memory");
    const char* wb = (const char*)wqkv + 13824 * h;
    const uint32_t wd = Lbase + OFF_W + 13824 * h;
    if (p == 0) { dma4(wb, wvA, wvA, wd); dma4(wb + 4096, wvM, wvB, wd + 4096); }
    uint32_t lx = lane;
    asm volatile("" : "+v"(lx));
    uint32_t xr[3];
#pragma unroll
    for (int j = 0; j < 3; ++j) {
        const uint32_t pos = 144u * h + 64u * j + lx;
        const uint32_t a = (pos * 781u) >> 16, p96 = (pos * 683u) >> 16;
        xr[j] = ((pos << 4) ^ ((p96 & 1u) << 5)) + 41664u * a + (pos >= 336u ? wrapY : 0u) + (org0 + 1344u * p + 8192u - 1024u * j);
    }
    {
        int y = 7 * wh + 9; if (y >= 224) y -= 224;
        int xx = 7 * (ww0 + (wv >> 1)) + 9; if (xx >= 224) xx -= 224;
        if (lane < 6) dma16nt(x, (uint32_t)((y * 224 + xx) * 192 + 96 * (wv & 1)) + 16 * lx, Lbase + OFF_XL + 96 * wv);
    }
    dma144nt(xm, xr[0], xr[1], xr[2], Lbase + OFF_X + p * XS + 2304 * h);
    if (p == 0) {
        dma4(wb + 8192, wvB, wvA, wd + 8192);
        dma96(wb + 12288, wvA, wd + 12288);
    }
    uint32_t msrc[2];
    bool mok[2];
#pragma unroll
    for (int j = 0; j < 2; ++j) {
        const int pos = 98 * h + 64 * j + lane;
        const int q = pos >> 3, mk = (pos & 7) ^ ((q >> 1) & 7);
        msrc[j] = 98 * q + (mk < 6 ? 16 * mk : 82);
        mok[j] = (mk < 7) && (j == 0 || lane < 34);
    }
    asm volatile("" :: "v"(msrc[0]), "v"(msrc[1]));
    auto load_mask = [&](int i) {
        const char* mb = (const char*)mask + (size_t)(4 * blk + i) * 4802;
        const uint32_t dst = Lbase + OFF_MASK + i * MS + 98 * 16 * h;
        if (mok[0]) dma16nt(mb, msrc[0], dst);
        if (mok[1]) dma16nt(mb, msrc[1], dst + 1024);
    };
    const float onec = ((m & 3) >= 2) ? 1.0f : 0.0f;
    const f32x4 ONES = {onec, onec, onec, onec};
    const f32x4 ZERO = {0.f, 0.f, 0.f, 0.f};
    const float qsc = (g & 1) ? 1.0f : QSCALE;
    lds_ptr QL = L + OFF_QL + wv * 256;
    lds_ptr CNT = L + OFF_CNT;
    const uint32_t cnt_addr = Lbase + OFF_CNT;
    lds_ptr wab, wc, wv1;
    {
        const int q4 = m >> 2, p4 = m & 3, sx = 32 * (g & 1);
        lds_ptr rb = L + OFF_W + 576 * (8 * g + q4);
        wab = rb + ((48 * h + 8 * p4) ^ sx);
        wc = rb + ((48 * h + 32 + 8 * (p4 >> 1)) ^ sx) + 192 * (p4 & 1);
        wv1 = rb + 384 + ((48 * h + 32 + 8 * (p4 & 1)) ^ sx);
    }
    const int bperm = 4 * (16 * g + 2 * (m >> 2) + (m & 1));
    const uint32_t bmask = ((m & 3) < 2) ? 0xffffffffu : 0u;
    u32x4 IHL;
    {
        const uint32_t val = (g < 2) ? 0x3FB9u : 0xBB2Cu;
        const int j = m - 8 * (g & 1);
#pragma unroll
        for (int r = 0; r < 4; ++r) IHL[r] = (j == 2 * r) ? val : (j == 2 * r + 1) ? (val << 16) : 0u;
    }
    lds_ptr q3a, mLa, qa, osw, oLw, xa, woa0, woa2, PLw, PLr;
    uint32_t ma0;
    {
        const int vsw = (m >> 1) & 7;
        lds_ptr M = L + OFF_MASK + p * MS;
        ma0 = Lbase + OFF_MASK + p * MS + 128 * m + 16 * ((g & 1) ^ vsw);
        q3a = M + 128 * m + (g < 2 ? 16 * (6 ^ vsw) + 12 : 16 * (7 ^ vsw) + 4 * h);
        mLa = M + 128 * 48 + (lane < 48 ? 2 * lane : 110);
        qa = QL + 64 * p + 16 * g;
        osw = L + OFF_OB + p * OBS + lane * 16 + 4 * (h ^ (g & 1));
        oLw = L + OFF_OL + p * 3072 + lane * 16 + 4 * (h ^ (g & 1));
        xa = L + OFF_X + p * XS + 192 * m + ((16 * g) ^ (((m >> 3) & 1) << 5));
        const int q4 = m >> 2, p4 = m & 3;
        const int rb = 24 * ((q4 >> 1) ^ (g & 1)) + (q4 & 1);
        const int cb = (role < 3) ? 64 * role : 0;
        woa0 = L + OFF_WO + (rb + 4 * g) * 192 + 16 * p4 + cb;
        woa2 = L + OFF_WO + (rb + 16 + 2 * g) * 192 + 16 * p4 + cb;
        PLw = L + OFF_PL + wv * 128 + 2 * lane;
        PLr = L + OFF_PL + wv * 128 + 8 * g;
    }
    uint32_t so[3];
#pragma unroll
    for (int qt = 0; qt < 3; ++qt) {
        const uint32_t t = 16u * qt + m, a = (t * 37u) >> 8;
        so[qt] = 192u * t + 41664u * a + (a >= 4u ? wrapY : 0u) + 16u * g + (uint32_t)(64 * role) + (org0 + 1344u * p);
    }
    asm volatile("" : "+v"(so[0]), "+v"(so[1]), "+v"(so[2]), "+v"(IHL), "+v"(ma0), "+v"(q3a), "+v"(mLa));
    asm volatile("" : "+v"(qa), "+v"(osw), "+v"(xa), "+v"(woa0), "+v"(woa2));
    asm volatile("" : "+v"(wab), "+v"(wc), "+v"(wv1));

    if (p == 1) {
        asm volatile("s_waitcnt vmcnt(7)" : "+v"(rp0), "+v"(rp1), "+v"(rp2) :: "memory");
        auto entry = [&](uint32_t off, u32x2 r) {
            *(LAS f32x4*)(L + OFF_RPB + off) = f32x4{bflo(r[0]) * LOG2E, bfhi(r[0]) * LOG2E, bflo(r[1]) * LOG2E, bfhi(r[1]) * LOG2E};
        };
        entry(2u * rsrc, rp0); entry(2u * rsrc + 1024u, rp1); entry(2u * rsrc3, rp2);
        asm volatile("s_waitcnt vmcnt(4)" : "+v"(ivA), "+v"(ivB), "+v"(ivC) :: "memory");
        lds_ptr T = L + OFF_RPB;
        u32x4 bA[4], bB[4], bC[4];
#pragma unroll
        for (int r = 0; r < 4; ++r) bA[r] = lds_r128(T + 16 * ivA[r]);
#pragma unroll
        for (int r = 0; r < 4; ++r) bB[r] = lds_r128(T + 16 * ivB[r]);
#pragma unroll
        for (int r = 0; r < 4; ++r) bC[r] = lds_r128(T + 16 * ivC[r]);
        SCHED_FENCE();
        lds_ptr D = L + OFF_BH + 1024 * h + 16 * lane;
#pragma unroll
        for (int hb = 0; hb < 4; ++hb) lds_w128(D + 9216 * hb, u32x4{bA[0][hb], bA[1][hb], bA[2][hb], bA[3][hb]});
#pragma unroll
        for (int hb = 0; hb < 4; ++hb) lds_w128(D + 9216 * hb + 4096, u32x4{bB[0][hb], bB[1][hb], bB[2][hb], bB[3][hb]});
        if (h == 0) {
#pragma unroll
            for (int hb = 0; hb < 4; ++hb) lds_w128(D + 9216 * hb + 8192, u32x4{bC[0][hb], bC[1][hb], bC[2][hb], bC[3][hb]});
        } else if (h == 1) {
            u32x4 pr;
#pragma unroll
            for (int hb = 0; hb < 4; ++hb) {
                const uint32_t bw = bC[3][hb];
                const float bv = __builtin_bit_cast(float, bw);
                const uint32_t hi = pk(bv, 0.f) & 0xffffu;
                pr[hb] = hi | (pk(bv - bflo(hi), 0.f) << 16);
            }
            const int a48 = 128 * lane + 16 * (7 ^ ((lane >> 1) & 7));
            if (lane < 48) {
#pragma unroll
                for (int i = 0; i < 4; ++i) lds_w128(L + OFF_MASK + i * MS + a48, pr);
            }
        } else if (h == 2) lds_w128(L + OFF_BL + 16 * lane, bC[3]);
        if (PPRO) __builtin_amdgcn_s_setprio(0);
    }
    WAIT_VM(0);
    BARRIER();
    PRIO_HOOK(0);

    u32x4 Wf[5][3];
    auto load_wf = [&](int s) {
        auto fr = [&](lds_ptr a) -> u32x4 {
            u32x2 lo = lds_tr(a + 18432 * s), hi = lds_tr(a + 18432 * s + 2304);
            return u32x4{lo[0], lo[1], hi[0], hi[1]};
        };
        Wf[0][s] = fr(wab);
        Wf[1][s] = fr(wab + 192);
        Wf[2][s] = fr(wc);
        Wf[3][s] = fr(wab + 384);
        const u32x4 t = fr(wv1);
#pragma unroll
        for (int r = 0; r < 4; ++r) Wf[4][s][r] = (uint32_t)__builtin_amdgcn_ds_bpermute(bperm, (int)t[r]) & bmask;
    };
    struct Acc { f32x4 a0, a1, a2, v0, v1; };
    auto proj_step = [&](Acc& A, u32x4 xf, int s, bool first) {
        A.a0 = mfma(Wf[0][s], xf, first ? ZERO : A.a0);
        A.a1 = mfma(Wf[1][s], xf, first ? ZERO : A.a1);
        A.a2 = mfma(Wf[2][s], xf, first ? ZERO : A.a2);
        A.v0 = mfma(xf, Wf[3][s], first ? ZERO : A.v0);
        A.v1 = mfma(xf, Wf[4][s], first ? ONES : A.v1);
    };
    auto proj_fin = [&](const Acc& A, u32x4& Q, u32x4& K, u32x2& V0, u32x2& V1) {
        const uint32_t c0 = pks(A.a2[0], A.a2[1], qsc), c1 = pks(A.a2[2], A.a2[3], qsc);
        const auto sw = __builtin_amdgcn_permlane16_swap(c0, c1, false, false);
        Q[0] = pks(A.a0[0], A.a0[1], QSCALE); Q[1] = pks(A.a0[2], A.a0[3], QSCALE); Q[2] = sw[0];
        K[0] = pk(A.a1[0], A.a1[1]); K[1] = pk(A.a1[2], A.a1[3]); K[2] = sw[1];
        V0 = u32x2{pk(A.v0[0], A.v0[1]), pk(A.v0[2], A.v0[3])};
        V1 = u32x2{pk(A.v1[0], A.v1[1]), pk(A.v1[2], A.v1[3])};
    };
    Frags F;
    F.Q[0][3] = F.Q[1][3] = F.Q[2][3] = 0u;
    F.K[0][3] = F.K[1][3] = F.K[2][3] = 0u;
    F.K[3][3] = (g == 0) ? 0x3FB90000u : (g == 1) ? 0xBB2C0000u : (g == 2) ? 0x3F803F80u : 0u;
    u32x2 V0L, V1L;
    f32x4 bias[3][3];
    float biasL;
    u32x2 V0[3], V1[3];
    auto set_vt = [&]() {
        F.VT[0][0] = u32x4{V0[0][0], V0[0][1], V0[1][0], V0[1][1]};
        F.VT[0][1] = u32x4{V0[2][0], V0[2][1], V0L[0], V0L[1]};
        F.VT[1][0] = u32x4{V1[0][0], V1[0][1], V1[1][0], V1[1][1]};
        F.VT[1][1] = u32x4{V1[2][0], V1[2][1], V1L[0], V1L[1]};
    };
    auto proj_tile = [&](lds_ptr a, u32x4& Q, u32x4& K, u32x2& v0, u32x2& v1) {
        u32x4 xf0 = lds_r128(a), xf1 = lds_r128(a + 64), xf2 = lds_r128(a + 128);
        Acc A;
        proj_step(A, xf0, 0, true); proj_step(A, xf1, 1, false); proj_step(A, xf2, 2, false);
        proj_fin(A, Q, K, v0, v1);
    };

    {
        lds_ptr xl = L + OFF_XL + 192 * (m >> 2) + 16 * g;
        const u32x4 xl0 = lds_r128(xl), xl1 = lds_r128(xl + 64), xl2 = lds_r128(xl + 128);
        load_wf(0); load_wf(1); load_wf(2);
        const u32x4 x00 = lds_r128(xa), x01 = lds_r128(xa + 64), x02 = lds_r128(xa + 128);
        load_mask(p);
        {
            uint32_t v0 = xr[0], v1 = xr[1], v2 = xr[2];
            if (p == 1 && ww0 == 28) {
    #pragma unroll
                for (int j = 0; j < 3; ++j) {
                    const uint32_t pos = 144u * h + 64u * j + lane;
                    const uint32_t a = (pos * 781u) >> 16;
                    const uint32_t d = (pos - 84u * a >= 48u) ? (uint32_t)-43008 : 0u;
                    if (j == 0) v0 += d; else if (j == 1) v1 += d; else v2 += d;
                }
            }
            dma144nt(xm + 2688, v0, v1, v2, Lbase + OFF_X + (p + 2) * XS + 2304 * h);
        }
        load_mask(p + 2);
        Acc A, B;
        proj_step(A, xl0, 0, true); proj_step(A, xl1, 1, false); proj_step(A, xl2, 2, false);
        {
            lds_ptr B = L + OFF_BH + 9216 * h + 16 * lane;
#pragma unroll
            for (int qt = 0; qt < 3; ++qt)
#pragma unroll
                for (int kt = 0; kt < 3; ++kt) bias[qt][kt] = lds_rf128(B + 1024 * (3 * qt + kt));
            biasL = lds_rf(L + OFF_BL + 16 * lane + 4 * h);
        }
        arrive(cnt_addr, 1u);
        proj_step(B, x00, 0, true); proj_step(B, x01, 1, false); proj_step(B, x02, 2, false);
        u32x4 QLr = {0u, 0u, 0u, 0u};
        proj_fin(A, QLr, F.K[3], V0L, V1L);
        if ((m & 3) == 0) lds_w128(QL + (m >> 2) * 64 + g * 16, QLr);
        proj_fin(B, F.Q[0], F.K[0], V0[0], V1[0]);
        proj_tile(xa + 3072, F.Q[1], F.K[1], V0[1], V1[1]);
        proj_tile(xa + 6144, F.Q[2], F.K[2], V0[2], V1[2]);
        set_vt();
    }
    WAIT_VM(2);
    arrive(cnt_addr, 16u << (4 * p));
    PRIO_HOOK(1);
    wait_fields(CNT, peek(CNT), 0xFu | (0xF0u << (4 * p)), 8u | (0x40u << (4 * p)));

    auto wo_frag = [&](int colb, int s) -> u32x4 {
        lds_ptr a = (s == 2 ? woa2 : woa0 + (s == 1 ? 384 : 0)) + colb;
        u32x2 lo = lds_tr(a), hi = lds_tr(a + 48 * 192);
        return u32x4{lo[0], lo[1], hi[0], hi[1]};
    };
    struct Pre { u32x4 ql, mk[3][3]; uint32_t q3[3], mL; };
    auto fetch = [&](auto itc, Pre& P) {
        constexpr int it = decltype(itc)::value;
        constexpr int MO = 2 * MS * it;
        P.ql = lds_r128(qa + 128 * it);
        P.mL = lds_r16(mLa + MO);
        uint32_t a0 = ma0;
        asm volatile("" : "+v"(a0));
        lds_ptr ma0p = (lds_ptr)(uintptr_t)a0, ma1p = (lds_ptr)(uintptr_t)(a0 ^ 32u), ma2p = (lds_ptr)(uintptr_t)(a0 ^ 64u);
#pragma unroll
        for (int tt = 0; tt < 3; ++tt) P.q3[tt] = lds_r32(q3a + MO + 2048 * tt);
#pragma unroll
        for (int tt = 0; tt < (it == 0 ? NPRE0 : 3); ++tt) {
            P.mk[tt][0] = lds_r128(ma0p + MO + 2048 * tt);
            P.mk[tt][1] = lds_r128(ma1p + MO + 2048 * tt);
            P.mk[tt][2] = lds_r128(ma2p + MO + 2048 * tt);
        }
    };
    auto window = [&](auto itc, Pre& P) {
        constexpr int it = decltype(itc)::value;
        constexpr int OO = 2 * OBS * it;
        const int i = p + 2 * it;
        uint32_t o[4][3];
        const f32x4 CW = {(g == i) ? 0.f : NEG_BIG, 0.f, 0.f, 0.f};
        const bool validL = (lane < 48) || (lane == 48 + 4 * i);
        uint32_t sb[4];
        asm volatile("" : "+v"(P.mL));
        const float cL = validL ? __builtin_fmaf(bflo(P.mL), LOG2E, biasL) : NEG_BIG;
        auto check = [&]() -> bool {
            const uint32_t lo = min(min(sb[0], sb[1]), min(sb[2], sb[3])), hi = max(max(sb[0], sb[1]), max(sb[2], sb[3]));
            const bool bad = (lo < 0x2B800000u) || (hi > 0x53800000u);
            return __builtin_amdgcn_ballot_w64(bad) != 0ull;
        };
        {
#pragma unroll
            for (int tt = 0; tt < 3; ++tt) F.Q[tt][3] = P.q3[tt];
            Sc S0, S1, S2;
            left_scores<false>(F, cL, validL, P.ql, PLw);
            const PLd pl = left_ld(PLr);
            scores(F, F.Q[0], bias[0], CW, P.mk[0], IHL, S0);
            if (it == 0) {
                uint32_t a0 = ma0;
                asm volatile("" : "+v"(a0));
                lds_ptr ma0p = (lds_ptr)(uintptr_t)a0, ma1p = (lds_ptr)(uintptr_t)(a0 ^ 32u), ma2p = (lds_ptr)(uintptr_t)(a0 ^ 64u);
#pragma unroll
                for (int tt = NPRE0; tt < 3; ++tt) {
                    P.mk[tt][0] = lds_r128(ma0p + 2048 * tt);
                    P.mk[tt][1] = lds_r128(ma1p + 2048 * tt);
                    P.mk[tt][2] = lds_r128(ma2p + 2048 * tt);
                }
            }
            SCHED_FENCE();
            scores(F, F.Q[1], bias[1], CW, P.mk[1], IHL, S1);
            exp_pv(F, S0, o[0], sb[0]);
            scores(F, F.Q[2], bias[2], CW, P.mk[2], IHL, S2);
            exp_pv(F, S1, o[1], sb[1]);
            left_pv(F, pl, o[3], sb[3]);
            exp_pv(F, S2, o[2], sb[2]);
        }
        if (__builtin_expect(check(), 0)) {
            lds_ptr M = L + OFF_MASK + i * MS;
#pragma unroll
            for (int qt = 0; qt < 3; ++qt) {
                Sc S;
                u32x4 Q = F.Q[qt];
                Q[3] = 0u;
                scores_safe(F, Q, bias[qt], CW, M + 2048 * qt, S);
                exp_pv(F, S, o[qt], sb[qt]);
            }
            left_scores<true>(F, cL, validL, lds_r128(qa + 128 * it), PLw);
            left_pv(F, left_ld(PLr), o[3], sb[3]);
        }
#pragma unroll
        for (int qt = 0; qt < 3; ++qt)
#pragma unroll
            for (int s = 0; s < 3; ++s) lds_w32(osw + OO + qt * 3072 + s * 1024, o[qt][s]);
        if (m == it) {
#pragma unroll
            for (int s = 0; s < 3; ++s) lds_w32(oLw + s * 1024, o[3][s]);
        }
        if (it == 0) WAIT_VM(0);
        arrive(cnt_addr, (it == 0 ? 0x1000u : 0x100000u) << (4 * p));
    };
    auto out_store = [&](const f32x4& c0, const f32x4& c1, uint32_t off) {
        u32x4 r = {pk(c0[0], c0[1]), pk(c0[2], c0[3]), pk(c1[0], c1[1]), pk(c1[2], c1[3])};
        gstore128(out, off, r);
    };

    Pre P;
    fetch(std::integral_constant<int, 0>{}, P);
    SCHED_FENCE();
    if (p == 0) {
        dma144((const char*)wo + 4608 * h, 16u * lane, Lbase + OFF_WO + 4608 * h);
        dma144((const char*)wo + 4608 * h + 2304, 16u * lane, Lbase + OFF_WO + 4608 * h + 2304);
    }
    window(std::integral_constant<int, 0>{}, P);
    PRIO_HOOK(2);
    proj_tile(xa + 2 * XS, F.Q[0], F.K[0], V0[0], V1[0]);
    proj_tile(xa + 2 * XS + 3072, F.Q[1], F.K[1], V0[1], V1[1]);
    proj_tile(xa + 2 * XS + 6144, F.Q[2], F.K[2], V0[2], V1[2]);
    set_vt();
    PRIO_HOOK(3);
    wait_fields(CNT, peek(CNT), p ? 0xFF000u : 0xF000u, p ? 0x44000u : 0x4000u);
    lds_ptr ord = (lds_ptr)(uintptr_t)((uint32_t)(uintptr_t)osw & ~15u);
    {
        u32x4 of[3][3], w[6];
        if (role < 3) {
#pragma unroll
            for (int s = 0; s < 3; ++s) {
                w[s] = wo_frag(0, s); w[3 + s] = wo_frag(8, s);
#pragma unroll
                for (int qt = 0; qt < 3; ++qt) of[qt][s] = lds_r128(ord + qt * 3072 + s * 1024);
            }
        }
        fetch(std::integral_constant<int, 1>{}, P);
        SCHED_FENCE();
        if (role < 3) {
            f32x4 c0[3], c1[3];
#pragma unroll
            for (int s = 0; s < 3; ++s)
#pragma unroll
                for (int qt = 0; qt < 3; ++qt) {
                    c0[qt] = mfma(w[s], of[qt][s], s ? c0[qt] : ZERO);
                    c1[qt] = mfma(w[3 + s], of[qt][s], s ? c1[qt] : ZERO);
                }
#pragma unroll
            for (int qt = 0; qt < 3; ++qt) out_store(c0[qt], c1[qt], so[qt]);
        }
    }
    PRIO_HOOK(4);
    window(std::integral_constant<int, 1>{}, P);
    PRIO_HOOK(5);
    if (role < 3) {
        uint32_t sx[3];
#pragma unroll
        for (int qt = 0; qt < 3; ++qt) sx[qt] = so[qt] + 2688u;
        if (ww0 + p + 2 == 31) {
            uint32_t mo = lane;
            asm volatile("" : "+v"(mo));
            mo &= 15u;
#pragma unroll
            for (int qt = 0; qt < 3; ++qt) {
                const uint32_t t = 16u * qt + mo, a = (t * 37u) >> 8;
                sx[qt] += (t - 7u * a >= 4u) ? (uint32_t)-43008 : 0u;
            }
        }
        u32x4 w[6];
#pragma unroll
        for (int s = 0; s < 3; ++s) { w[s] = wo_frag(0, s); w[3 + s] = wo_frag(8, s); }
        wait_fields(CNT, peek(CNT), 0xF00000u << (4 * p), 0x400000u << (4 * p));
        u32x4 of[3][3];
#pragma unroll
        for (int s = 0; s < 3; ++s)
#pragma unroll
            for (int qt = 0; qt < 3; ++qt) of[qt][s] = lds_r128(ord + 2 * OBS + qt * 3072 + s * 1024);
        f32x4 c0[3], c1[3];
#pragma unroll
        for (int s = 0; s < 3; ++s)
#pragma unroll
            for (int qt = 0; qt < 3; ++qt) {
                c0[qt] = mfma(w[s], of[qt][s], s ? c0[qt] : ZERO);
                c1[qt] = mfma(w[3 + s], of[qt][s], s ? c1[qt] : ZERO);
            }
#pragma unroll
        for (int qt = 0; qt < 3; ++qt) out_store(c0[qt], c1[qt], sx[qt]);
    } else {
        u32x4 w[3][6];
#pragma unroll
        for (int i2 = 0; i2 < 3; ++i2)
#pragma unroll
            for (int s = 0; s < 3; ++s) { w[i2][s] = wo_frag(64 * i2, s); w[i2][3 + s] = wo_frag(64 * i2 + 8, s); }
        int y = 7 * wh + 9; if (y >= 224) y -= 224;
        int xx = 7 * (ww0 + p + 2 * (m & 1)) + 9; if (xx >= 224) xx -= 224;
        const uint32_t dst = (uint32_t)((y * 224 + xx) * 192 + 16 * g);
        wait_fields(CNT, peek(CNT), 0xF00000u << (4 * p), 0x400000u << (4 * p));
        lds_ptr orr = L + OFF_OL + p * 3072 + lane * 16;
        u32x4 of0 = lds_r128(orr), of1 = lds_r128(orr + 1024), of2 = lds_r128(orr + 2048);
        f32x4 c0[3], c1[3];
#pragma unroll
        for (int i2 = 0; i2 < 3; ++i2) { c0[i2] = mfma(w[i2][0], of0, ZERO); c1[i2] = mfma(w[i2][3], of0, ZERO); }
#pragma unroll
        for (int i2 = 0; i2 < 3; ++i2) { c0[i2] = mfma(w[i2][1], of1, c0[i2]); c1[i2] = mfma(w[i2][4], of1, c1[i2]); }
#pragma unroll
        for (int i2 = 0; i2 < 3; ++i2) { c0[i2] = mfma(w[i2][2], of2, c0[i2]); c1[i2] = mfma(w[i2][5], of2, c1[i2]); }
        if (m < 2) {
#pragma unroll
            for (int i2 = 0; i2 < 3; ++i2) out_store(c0[i2], c1[i2], dst + 64 * i2);
        }
    }
}

extern "C" void kernel_launch(void* const* d_in, const int* in_sizes, int n_in, void* d_out, int out_size, void* d_ws,
                              size_t ws_size, hipStream_t stream) {
    (void)in_sizes; (void)n_in; (void)out_size; (void)d_ws; (void)ws_size;
    (void)hipFuncSetAttribute((const void*)wattn_kernel, hipFuncAttributeMaxDynamicSharedMemorySize, LDS_TOTAL);
    wattn_kernel<<<256, 512, LDS_TOTAL, stream>>>((const uint16_t*)d_in[0], (const uint16_t*)d_in[1], (const uint16_t*)d_in[2],
                                                  (const uint16_t*)d_in[3], (const int*)d_in[4], (const uint16_t*)d_in[5],
                                                  (uint16_t*)d_out);
}
```
